```python
import torch

def _softmax(x, axis=-1):
    return torch.softmax(x, dim=axis)

def _gelu(x):
    return torch.nn.functional.gelu(x, approximate='tanh')

def _silu(x):
    return torch.nn.functional.silu(x)

def _sigmoid(x):
    return torch.sigmoid(x)

def _layernorm(x, axis=-1, eps=1e-05):
    m = x.mean(dim=axis, keepdim=True)
    v = x.var(dim=axis, keepdim=True, unbiased=False)
    return (x - m) / torch.sqrt(v + eps)

def _scatter_add(tgt, idx, src, axis=0):
    return tgt.clone().index_add_(axis, idx, src)

def _segment_sum(src, seg, n):
    out = torch.zeros((n,) + src.shape[1:], dtype=src.dtype)
    return out.index_add_(0, seg, src)

def _pad(x, pw):
    flat = tuple((v for pair in reversed(pw) for v in pair))
    return torch.nn.functional.pad(x, flat)
kernel_name = 'adaLN_cond__sliding_local_dilated__dense_activation_silu__prompt'

def setup_inputs():

    _w = lambda a: (lambda t: t.to(torch.bfloat16) if t.is_floating_point() else t)(torch.as_tensor(a))

    def _A_setup():
        B, S, D = (1, 2048, 512)
        return {'x': _w((lambda *s: torch.randn(*s, dtype=torch.bfloat16) * 0.05)(B, S, D)), 'wqkv': _w((lambda *s: torch.randn(*s, dtype=torch.bfloat16) * 0.05)(D, 3 * D)), 'wo': _w((lambda *s: torch.randn(*s, dtype=torch.bfloat16) * 0.05)(D, D))}
    _A_in = _A_setup()

    def _B_setup():
        B, S, D, F = (1, 2048, 512, 1376)
        return {'x': _w((lambda *s: torch.randn(*s, dtype=torch.bfloat16) * 0.05)(B, S, D)), 'w1': _w((lambda *s: torch.randn(*s, dtype=torch.bfloat16) * 3.2)(D, F) * D ** (-0.5)), 'w2': _w((lambda *s: torch.randn(*s, dtype=torch.bfloat16) * 0.4)(F, D) * F ** (-0.5))}
    _B_in = _B_setup()
    return {'A_x': _A_in['x'], 'A_wqkv': _A_in['wqkv'], 'A_wo': _A_in['wo'], 'B_x': _B_in['x'], 'B_w1': _B_in['w1'], 'B_w2': _B_in['w2'], 'c': _w((lambda *s: torch.randn(*s, dtype=torch.bfloat16) * 0.1)(1, 512) * 8.0), 'w_mod': _w((lambda *s: torch.randn(*s, dtype=torch.bfloat16) * 0.1)(512, 6 * 512) * 512 ** (-0.5) * 8.0)}

def reference(A_x, A_wqkv, A_wo, B_x, B_w1, B_w2, c, w_mod):

    def _A_ref(x, wqkv, wo):
        B, S, D = x.shape
        H = 32
        Dh = D // H
        W, dil = (8, 2)
        qkv = (lambda x, *d: x.permute(*d))((x @ wqkv).reshape(B, S, 3, H, Dh), 2, 0, 3, 1, 4)
        q, k, v = (qkv[0], qkv[1], qkv[2])
        scale = Dh ** (-0.5)
        pos = torch.arange(S)
        off = torch.arange(W) * dil
        idx = pos[:, None] - off[None, :]
        mask = idx >= 0
        idx_c = torch.where(mask, idx, 0).reshape(-1)
        kg = k[:, :, idx_c, :].reshape(B, H, S, W, Dh)
        vg = v[:, :, idx_c, :].reshape(B, H, S, W, Dh)
        logits = torch.einsum('bhsd,bhswd->bhsw', q, kg) * scale
        logits = torch.where(mask[None, None, :, :], logits, -1000000000.0)
        attn = _softmax(logits, axis=-1)
        o = torch.einsum('bhsw,bhswd->bhsd', attn, vg)
        return (lambda x, *d: x.permute(*d))(o, 0, 2, 1, 3).reshape(B, S, D) @ wo

    def _B_ref(x, w1, w2):
        h = torch.einsum('bsd,df->bsf', x, w1)
        h = _silu(h)
        out = torch.einsum('bsf,fd->bsd', h, w2)
        return out
    x = A_x
    mod = (_silu(c) @ w_mod).reshape(c.shape[0], 6, 1, -1)
    sa, ca, ga, sb, cb, gb = (mod[:, 0].clone(), mod[:, 1].clone(), mod[:, 2].clone(), mod[:, 3].clone(), mod[:, 4].clone(), mod[:, 5].clone())
    h = _layernorm(x, axis=-1) * (1 + ca) + sa
    x = x + ga * _A_ref(h, A_wqkv, A_wo)
    h = _layernorm(x, axis=-1) * (1 + cb) + sb
    x = x + gb * _B_ref(h, B_w1, B_w2)
    return x
```

```cpp
#include <hip/hip_runtime.h>
#include <hip/hip_bf16.h>
#include <stdint.h>

typedef unsigned short u16;
typedef short bf16x8 __attribute__((ext_vector_type(8)));
typedef float f32x4 __attribute__((ext_vector_type(4)));

__device__ __forceinline__ float bf2f(u16 b) { return __builtin_bit_cast(float, (unsigned)b << 16); }
__device__ __forceinline__ u16 f2bf(float f) {
  __hip_bfloat16 h = __float2bfloat16(f);
  return __builtin_bit_cast(u16, h);
}

constexpr int S = 2048, D = 512, F = 1376;

__global__ void k_mod(const u16* __restrict__ c, const u16* __restrict__ wmod, float* __restrict__ mod) {
  int n = blockIdx.x * blockDim.x + threadIdx.x;
  __shared__ float sc[512];
  for (int k = threadIdx.x; k < 512; k += blockDim.x) {
    float v = bf2f(c[k]);
    float s = v / (1.f + __expf(-v));
    sc[k] = bf2f(f2bf(s));
  }
  __syncthreads();
  float acc = 0.f;
  for (int k = 0; k < 512; ++k) acc += sc[k] * bf2f(wmod[(size_t)k * 3072 + n]);
  mod[n] = bf2f(f2bf(acc));
}

__global__ void k_ln(const u16* __restrict__ x, const float* __restrict__ svec, const float* __restrict__ cvec,
                     u16* __restrict__ h) {
  int row = blockIdx.x * 4 + (threadIdx.x >> 6);
  int lane = threadIdx.x & 63;
  const u16* xr = x + (size_t)row * D;
  float v[8];
  float s = 0.f;
  for (int j = 0; j < 8; ++j) { v[j] = bf2f(xr[lane * 8 + j]); s += v[j]; }
  for (int o = 32; o; o >>= 1) s += __shfl_xor(s, o);
  float m = s * (1.f / D);
  float q = 0.f;
  for (int j = 0; j < 8; ++j) { float d = v[j] - m; q += d * d; }
  for (int o = 32; o; o >>= 1) q += __shfl_xor(q, o);
  float r = rsqrtf(q * (1.f / D) + 1e-5f);
  for (int j = 0; j < 8; ++j) {
    int k = lane * 8 + j;
    h[(size_t)row * D + k] = f2bf((v[j] - m) * r * (1.f + cvec[k]) + svec[k]);
  }
}

template <int EPI>
__global__ __launch_bounds__(256) void k_gemm(const u16* __restrict__ A, const u16* __restrict__ B, u16* __restrict__ C,
                                               int N, int K, const u16* __restrict__ res, const float* __restrict__ g) {
  __shared__ u16 As[64][40];
  __shared__ u16 Bs[64][40];
  int bm = blockIdx.y * 64, bn = blockIdx.x * 64;
  int t = threadIdx.x, w = t >> 6, lane = t & 63;
  int wm = (w >> 1) * 32, wn = (w & 1) * 32;
  f32x4 acc[2][2] = {};
  for (int k0 = 0; k0 < K; k0 += 32) {
    {
      int r = t >> 2, cc = (t & 3) * 8;
      for (int j = 0; j < 8; ++j) As[r][cc + j] = A[(size_t)(bm + r) * K + k0 + cc + j];
      int kk = t >> 3, nn = (t & 7) * 8;
      for (int j = 0; j < 8; ++j) {
        int n = bn + nn + j;
        Bs[nn + j][kk] = n < N ? B[(size_t)(k0 + kk) * N + n] : (u16)0;
      }
    }
    __syncthreads();
    bf16x8 a[2], b[2];
    for (int i = 0; i < 2; ++i) {
      for (int j = 0; j < 8; ++j) {
        a[i][j] = As[wm + i * 16 + (lane & 15)][(lane >> 4) * 8 + j];
        b[i][j] = Bs[wn + i * 16 + (lane & 15)][(lane >> 4) * 8 + j];
      }
    }
    for (int i = 0; i < 2; ++i)
      for (int j = 0; j < 2; ++j) acc[i][j] = __builtin_amdgcn_mfma_f32_16x16x32_bf16(a[i], b[j], acc[i][j], 0, 0, 0);
    __syncthreads();
  }
  for (int i = 0; i < 2; ++i)
    for (int j = 0; j < 2; ++j)
      for (int r = 0; r < 4; ++r) {
        int row = bm + wm + i * 16 + (lane >> 4) * 4 + r;
        int col = bn + wn + j * 16 + (lane & 15);
        if (col < N) {
          float v = acc[i][j][r];
          if (EPI == 1) { v = bf2f(f2bf(v)); v = v / (1.f + __expf(-v)); }
          if (EPI == 2) { v = bf2f(res[(size_t)row * N + col]) + g[col] * v; }
          C[(size_t)row * N + col] = f2bf(v);
        }
      }
}

__global__ void k_attn(const u16* __restrict__ qkv, u16* __restrict__ o) {
  int idx = blockIdx.x * blockDim.x + threadIdx.x;
  int s = idx >> 5, h = idx & 31;
  float q[16];
  for (int d = 0; d < 16; ++d) q[d] = bf2f(qkv[(size_t)s * 1536 + h * 16 + d]);
  float lg[8];
  float mx = -3.0e38f;
  for (int w = 0; w < 8; ++w) {
    int p = s - 2 * w;
    float a = 0.f;
    if (p >= 0) {
      for (int d = 0; d < 16; ++d) a += q[d] * bf2f(qkv[(size_t)p * 1536 + 512 + h * 16 + d]);
      a *= 0.25f;
    } else a = -1e9f;
    lg[w] = a;
    mx = fmaxf(mx, a);
  }
  float sum = 0.f;
  for (int w = 0; w < 8; ++w) { lg[w] = __expf(lg[w] - mx); sum += lg[w]; }
  float inv = 1.f / sum;
  for (int d = 0; d < 16; ++d) {
    float a = 0.f;
    for (int w = 0; w < 8; ++w) {
      int p = s - 2 * w; if (p < 0) p = 0;
      a += lg[w] * inv * bf2f(qkv[(size_t)p * 1536 + 1024 + h * 16 + d]);
    }
    o[(size_t)s * 512 + h * 16 + d] = f2bf(a);
  }
}

extern "C" void kernel_launch(void* const* d_in, const int* in_sizes, int n_in, void* d_out, int out_size, void* d_ws,
                              size_t ws_size, hipStream_t stream) {
  const u16* x = (const u16*)d_in[0];
  const u16* wqkv = (const u16*)d_in[1];
  const u16* wo = (const u16*)d_in[2];
  const u16* w1 = (const u16*)d_in[4];
  const u16* w2 = (const u16*)d_in[5];
  const u16* c = (const u16*)d_in[6];
  const u16* wmod = (const u16*)d_in[7];
  char* ws = (char*)d_ws;
  float* mod = (float*)ws;
  u16* h = (u16*)(ws + (1 << 20));
  u16* qkv = (u16*)(ws + (4 << 20));
  u16* o = (u16*)(ws + (12 << 20));
  u16* x1 = (u16*)(ws + (16 << 20));
  u16* hid = (u16*)(ws + (20 << 20));
  u16* out = (u16*)d_out;
  k_mod<<<3072 / 64, 64, 0, stream>>>(c, wmod, mod);
  k_ln<<<S / 4, 256, 0, stream>>>(x, mod + 0, mod + 512, h);
  k_gemm<0><<<dim3(1536 / 64, S / 64), 256, 0, stream>>>(h, wqkv, qkv, 1536, 512, nullptr, nullptr);
  k_attn<<<S * 32 / 256, 256, 0, stream>>>(qkv, o);
  k_gemm<2><<<dim3(512 / 64, S / 64), 256, 0, stream>>>(o, wo, x1, 512, 512, x, mod + 1024);
  k_ln<<<S / 4, 256, 0, stream>>>(x1, mod + 1536, mod + 2048, h);
  k_gemm<1><<<dim3((F + 63) / 64, S / 64), 256, 0, stream>>>(h, w1, hid, F, 512, nullptr, nullptr);
  k_gemm<2><<<dim3(512 / 64, S / 64), 256, 0, stream>>>(hid, w2, out, 512, F, x1, mod + 2560);
}
```

```cpp
#include <hip/hip_runtime.h>
#include <stdint.h>

struct Srd {
  const void* p;
  uint32_t n;
  uint32_t f;
};
struct Args {
  Srd s[9];
};

__global__ __launch_bounds__(512, 2) void fused_block(Args a) {
  __shared__ __attribute__((aligned(16))) char lds[163840];
  unsigned tid = threadIdx.x;
  unsigned bid = blockIdx.x;
  auto ka = __builtin_amdgcn_kernarg_segment_ptr();
  asm volatile(R"ASM(
  s_load_dwordx16 s[20:35], s[0:1], 64
  s_load_dwordx4 s[36:39], s[0:1], 0x80
  s_load_dwordx16 s[4:19], s[0:1], 0
  v_lshrrev_b32 v8, 6, v0
  v_and_b32 v0, 63, v0
  v_readfirstlane_b32 s44, v8
  v_lshlrev_b32 v1, 4, v0
  s_and_b32 s55, s2, 7
  s_bfe_u32 s48, s2, 0x30003
  s_lshr_b32 s56, s2, 6
  s_lshl2_add_u32 s47, s55, s56
  s_lshr_b32 s57, s48, 1
  s_mul_i32 s3, s47, 6
  s_add_u32 s3, s3, s48
  s_cmp_gt_u32 s57, s56
  s_cselect_b32 s58, 2, 0
  s_sub_u32 s3, s3, s58
  s_xor_b32 s77, s57, s56
  v_lshrrev_b32 v21, 1, v0
  v_mul_u32_u24 v7, 0x1800, v21
  v_and_b32 v22, 16, v1
  v_add_u32 v7, v7, v22
  v_lshlrev_b32 v20, 1, v21
  s_lshl1_add_u32 s57, s3, s44
  s_and_b32 s57, s57, 7
  s_mul_i32 s55, s57, 0x60000
  s_lshl_b32 s56, s3, 5
  s_add_u32 s79, s55, s56
  s_lshl_b32 s57, s57, 7
  s_lshr_b32 s45, s44, 2
  s_and_b32 s46, s44, 3
  s_lshl_b32 s72, s47, 6
  s_lshl2_add_u32 s73, s46, s72
  s_add_u32 s73, s73, s45
  s_sub_i32 s73, s73, 16
  s_add_i32 s74, s73, 2
  s_max_i32 s73, s73, 0
  s_max_i32 s74, s74, 0
  s_lshl_b32 s73, s73, 10
  s_lshl_b32 s74, s74, 10
  s_lshl2_add_u32 s72, s44, s72
  s_lshl_b32 s72, s72, 10
  s_add_u32 s75, s72, 0x8000
  s_lshr_b32 s78, s2, 6
  s_lshl_b32 s58, s78, 8
  v_xor_b32 v6, s58, v1
  s_lshl_b32 s78, s78, 1
  s_waitcnt lgkmcnt(0)
  buffer_load_ushort v37, v20, s[28:31], s57 offen
  buffer_load_ushort v38, v20, s[28:31], s57 offen offset:64
  buffer_load_dword v39, off, s[36:39], 0 sc1
  s_lshr_b32 s31, s2, 6
  v_lshlrev_b32 v5, 3, v0
  s_cmp_eq_u32 s77, 0
  s_cselect_b32 s79, 0x40000000, s79
  s_add_u32 s56, s79, 0x30000
  buffer_load_dwordx4 v[8:11], v7, s[24:27], s79 offen
  buffer_load_dwordx4 v[12:15], v7, s[24:27], s56 offen
  v_lshlrev_b32 v23, 7, v0
  s_and_b32 s58, s48, 1
  s_lshl1_add_u32 s56, s47, s58
  s_cmp_eq_u32 s77, 0
  s_cselect_b32 s55, s44, 15
  s_mul_i32 s57, s56, 3
  s_add_u32 s57, s57, s55
  s_lshl_b32 s57, s57, 13
  s_cmp_lt_u32 s55, 3
  s_cselect_b32 s57, s57, 0x40000000
  buffer_load_dword v250, v23, s[8:11], s57 offen
  s_cmp_lg_u32 s55, 3
  s_cbranch_scc1 Lsk1_%=
  s_lshl_b32 s57, s56, 13
  buffer_load_dword v251, v23, s[12:15], s57 offen
Lsk1_%=:
  s_cmp_lg_u32 s55, 4
  s_cbranch_scc1 Lsk2_%=
  s_getpc_b64 s[60:61]
  s_and_b32 s57, s56, 7
  s_lshl_b32 s57, s57, 12
  v_and_b32 v252, 0xf80, v23
  v_add_u32 v252, s57, v252
  v_min_u32 v252, 20480, v252
  global_load_dword v252, v252, s[60:61]
Lsk2_%=:
  s_mov_b64 s[0:1], -1
  s_mov_b32 s53, -2
  s_mov_b32 s50, 0x7530
  s_lshr_b32 s58, s48, 1
  s_mul_i32 s54, s58, 11
  s_cmp_eq_u32 s48, 7
  s_cselect_b32 s58, 5, 6
  s_bitcmp1_b32 s48, 0
  s_cselect_b32 s58, s58, 0
  s_add_u32 s54, s54, s58
  s_mul_i32 s55, s47, 0x54000
  s_add_u32 s55, s55, 0x10000
  s_add_u32 s40, s36, s55
  s_addc_u32 s41, s37, 0
  s_mov_b32 s42, 0x54000
  s_mov_b32 s43, 0x20000
  v_lshrrev_b32 v32, 5, v0
  v_lshlrev_b32 v32, 1, v32
  v_bfe_u32 v33, v0, 4, 1
  v_lshl_add_u32 v32, v33, 3, v32
  v_bfe_u32 v33, v0, 3, 1
  v_add_u32 v32, v32, v33
  v_mul_u32_u24 v32, 0xc00, v32
  v_lshrrev_b32 v33, 4, v0
  v_lshlrev_b32 v33, 5, v33
  v_and_b32 v4, 0x70, v1
  v_xor_b32 v4, v4, v33
  v_add_u32 v4, v32, v4
  s_lshl_b32 s52, s45, 5
  s_lshr_b32 s55, s46, 1
  s_lshl4_add_u32 s52, s55, s52
  s_and_b32 s55, s46, 1
  s_lshl2_add_u32 s52, s55, s52
  s_mul_i32 s52, s52, 0xc00
  s_lshl_b32 s55, s48, 7
  s_add_u32 s52, s52, s55
  s_mul_i32 s51, s44, 0xc00
  v_lshrrev_b32 v32, 5, v0
  v_mul_u32_u24 v32, 0xc00, v32
  v_bfe_u32 v33, v0, 2, 3
  v_lshl_add_u32 v32, v33, 7, v32
  v_bfe_u32 v33, v0, 3, 2
  v_xor_b32 v33, s46, v33
  v_lshl_add_u32 v32, v33, 5, v32
  v_and_b32 v33, 3, v0
  v_lshl_add_u32 v52, v33, 3, v32
  v_add_u32 v53, 0xf000, v52
  v_add_u32 v54, 0x1e000, v52
  v_and_b32 v32, 15, v0
  v_lshrrev_b32 v33, 4, v0
  v_lshlrev_b32 v33, 8, v33
  s_mul_i32 s55, s45, 0x4100
  s_add_u32 s55, s55, 0x4100
  v_mul_u32_u24 v2, 0x410, v32
  v_add3_u32 v2, v2, v33, s55
  s_lshl_b32 s55, s45, 3
  s_xor_b32 s55, s55, 8
  v_xor_b32 v32, s55, v32
  v_mul_u32_u24 v3, 0x410, v32
  v_add_u32 v3, v3, v33
  v_and_b32 v236, 0xf0, v1
  v_lshrrev_b32 v237, 5, v0
  v_lshl_add_u32 v236, v237, 8, v236
  v_mul_u32_u24 v237, 0xf80, v237
  v_lshl_add_u32 v235, v0, 2, v237
  v_bfe_u32 v237, v0, 4, 1
  v_lshl_add_u32 v234, v237, 14, v236
  v_mul_u32_u24 v237, 0xb000, v237
  v_add_u32 v233, v237, v236
  s_lshl_b32 s55, s45, 15
  s_lshl2_add_u32 s56, s48, s46
  s_lshl_b32 s57, s56, 9
  s_add_u32 s97, s55, s57
  s_lshl_b32 s57, s46, 12
  s_add_u32 s55, s55, s57
  s_add_u32 s28, s55, 0x8000
  s_add_u32 s29, s55, 0xc000
  s_lshl_b32 s55, s47, 6
  s_add_u32 s55, s55, s45
  s_lshl_b32 s55, s55, 10
  s_lshl_b32 s56, s56, 5
  s_add_u32 s98, s55, s56
  s_mul_i32 s56, s46, 3
  s_lshl1_add_u32 s57, s54, s56
  s_lshl_b32 s57, s57, 9
  s_mul_i32 s58, s45, 0x16000
  s_add_u32 s57, s57, s58
  s_add_u32 s24, s57, 0x28000
  s_bitcmp1_b32 0x95, s48
  s_cselect_b32 s58, 4, 3
  s_cmp_lt_u32 s46, s58
  s_cselect_b32 s57, s57, 0x40000000
  s_add_u32 s25, s57, 0x28200
  s_add_u32 s26, s57, 0x28400
  v_bfe_u32 v236, v0, 4, 1
  v_bfe_u32 v237, v0, 2, 2
  v_lshl_add_u32 v237, v236, 3, v237
  v_lshlrev_b32 v232, 10, v237
  v_lshrrev_b32 v236, 4, v0
  v_xor_b32 v236, s46, v236
  v_lshlrev_b32 v236, 5, v236
  v_and_b32 v237, 48, v1
  v_xor_b32 v236, v236, v237
  v_add_u32 v232, v232, v236
  s_lshl_b32 s56, s45, 12
  s_lshl_b32 s57, s48, 7
  s_add_u32 s94, s56, s57
  s_lshl2_add_u32 s56, s46, s31
  s_lshl_b32 s56, s56, 15
  s_add_u32 s95, s56, s94
  s_lshl1_add_u32 s56, s46, s45
  s_lshl_b32 s96, s56, 11
  v_bfe_u32 v236, v0, 3, 1
  v_bfe_u32 v237, v0, 1, 2
  v_lshl_add_u32 v236, v236, 3, v237
  v_mul_u32_u24 v236, 0xac0, v236
  v_lshrrev_b32 v237, 4, v0
  v_lshl_add_u32 v236, v237, 5, v236
  v_and_b32 v237, 16, v1
  v_add_u32 v7, v236, v237
  s_and_b32 s55, s46, 1
  s_lshl1_add_u32 s55, s45, s55
  s_lshr_b32 s56, s46, 1
  s_lshl2_add_u32 s55, s55, s56
  s_mul_i32 s55, s55, 0x2b00
  s_lshl_b32 s56, s54, 6
  s_add_u32 s99, s55, s56
  v_mov_b32 v55, 0xff800000
  s_cmp_eq_u32 s47, 0
  s_cselect_b32 s30, 0, -1
  s_lshl_b32 s55, s48, 2
  s_lshr_b32 s55, 0x98754310, s55
  s_and_b32 s55, s55, 15
  s_mul_i32 s56, s31, 3
  s_add_u32 s56, s56, s55
  s_cmp_ge_u32 s56, 11
  s_cselect_b32 s55, 11, 0
  s_sub_u32 s27, s56, s55
  s_mul_i32 s55, s46, 11
  s_add_u32 s55, s55, s27
  s_lshl_b32 s55, s55, 15
  s_add_u32 s79, s55, s94
  s_barrier
  buffer_load_dwordx4 v[60:63], v6, s[4:7], s73 offen
  buffer_load_dwordx4 v[68:71], v6, s[4:7], s74 offen
  buffer_load_dwordx4 v[76:79], v6, s[4:7], s72 offen
  buffer_load_dwordx4 v[84:87], v6, s[4:7], s72 offen offset:2048
  buffer_load_dwordx4 v[92:95], v6, s[4:7], s72 offen offset:1024
  buffer_load_dwordx4 v[100:103], v6, s[4:7], s72 offen offset:3072
  s_add_u32 m0, s51, 0
  s_xor_b32 s59, s78, 0
  s_mul_i32 s59, s59, 0x30000
  s_add_u32 s59, s52, s59
  buffer_load_dwordx4 v4, s[8:11], s59 offen lds
  buffer_load_dwordx4 v4, s[8:11], s59 offen offset:1024 lds
  buffer_load_dwordx4 v4, s[8:11], s59 offen offset:2048 lds
  s_lshl_b32 s55, s44, 9
  s_add_u32 s72, s55, 0x1000
  s_add_u32 s55, s55, 0x26500
  v_add_u32 v238, s55, v5
  v_lshlrev_b32 v239, 1, v6
  v_add_u32 v239, 0x26500, v239
  s_add_u32 s73, s72, 0x1800
  s_lshl_b32 s74, s48, 8
  s_add_u32 s74, s74, 0x2000
  v_lshrrev_b32 v240, 5, v0
  v_mul_u32_u24 v240, 0x1700, v240
  v_add_u32 v240, v240, v5
  s_xor_b32 s55, s44, s31
  s_lshl_b32 s55, s55, 8
  s_add_u32 s55, s55, 0x27500
  v_lshl_add_u32 v241, v0, 2, s55
  v_lshlrev_b32 v242, 2, v0
  v_add_u32 v242, 0x27d00, v242
  s_cmp_eq_u32 s77, 0
  s_cbranch_scc1 Lsk3_%=
  v_and_b32 v24, 1, v0
  v_lshlrev_b32 v24, 8, v24
  v_and_b32 v25, 32, v0
  s_lshl2_add_u32 s55, s44, 0x27e00
  v_add3_u32 v24, v24, v25, s55
  v_bfe_u32 v25, v0, 4, 1
  v_lshl_add_u32 v24, v25, 6, v24
  s_waitcnt vmcnt(14)
  v_lshlrev_b32 v28, 16, v37
  s_waitcnt vmcnt(13)
  v_lshlrev_b32 v29, 16, v38
  v_mul_f32 v26, 0xbfb8aa3b, v28
  v_mul_f32 v27, 0xbfb8aa3b, v29
  v_exp_f32 v26, v26
  v_exp_f32 v27, v27
  v_add_f32 v26, 1.0, v26
  v_add_f32 v27, 1.0, v27
  v_rcp_f32 v26, v26
  v_rcp_f32 v27, v27
  v_mul_f32 v26, v26, v28
  v_mul_f32 v27, v27, v29
  v_cvt_pk_bf16_f32 v26, v26, v27
  s_waitcnt vmcnt(12)
  v_readfirstlane_b32 s49, v39
  s_add_u32 s49, s49, 1
  s_mov_b32 s55, 0x5040100
  s_waitcnt vmcnt(10)
  v_perm_b32 v16, v12, v8, s55
  s_mov_b32 s55, 0x7060302
  s_waitcnt vmcnt(10)
  v_perm_b32 v17, v12, v8, s55
  s_mov_b32 s55, 0x5040100
  s_waitcnt vmcnt(10)
  v_perm_b32 v18, v13, v9, s55
  s_mov_b32 s55, 0x7060302
  s_waitcnt vmcnt(10)
  v_perm_b32 v19, v13, v9, s55
  s_mov_b32 s55, 0x5040100
  s_waitcnt vmcnt(10)
  v_perm_b32 v20, v14, v10, s55
  s_mov_b32 s55, 0x7060302
  s_waitcnt vmcnt(10)
  v_perm_b32 v21, v14, v10, s55
  s_mov_b32 s55, 0x5040100
  s_waitcnt vmcnt(10)
  v_perm_b32 v22, v15, v11, s55
  s_mov_b32 s55, 0x7060302
  s_waitcnt vmcnt(10)
  v_perm_b32 v23, v15, v11, s55
  v_dot2_f32_bf16 v16, v16, v26, 0
  v_dot2_f32_bf16 v17, v17, v26, 0
  v_dot2_f32_bf16 v18, v18, v26, 0
  v_dot2_f32_bf16 v19, v19, v26, 0
  v_dot2_f32_bf16 v20, v20, v26, 0
  v_dot2_f32_bf16 v21, v21, v26, 0
  v_dot2_f32_bf16 v22, v22, v26, 0
  v_dot2_f32_bf16 v23, v23, v26, 0
  v_permlane32_swap_b32 v16, v17
  v_permlane32_swap_b32 v18, v19
  v_permlane32_swap_b32 v20, v21
  v_permlane32_swap_b32 v22, v23
  v_add_f32 v16, v16, v17
  v_add_f32 v18, v18, v19
  v_add_f32 v20, v20, v21
  v_add_f32 v22, v22, v23
  v_permlane16_swap_b32 v16, v18
  s_nop 0
  v_permlane16_swap_b32 v20, v22
  v_add_f32 v16, v16, v18
  v_add_f32 v20, v20, v22
  s_nop 0
  v_add_f32_dpp v16, v16, v16 row_ror:8 row_mask:0xf bank_mask:0xf
  v_add_f32_dpp v20, v20, v20 row_ror:8 row_mask:0xf bank_mask:0xf
  s_nop 0
  v_add_f32_dpp v16, v16, v16 row_ror:4 row_mask:0xf bank_mask:0xf
  v_add_f32_dpp v20, v20, v20 row_ror:4 row_mask:0xf bank_mask:0xf
  s_nop 0
  v_add_f32_dpp v16, v16, v16 row_ror:2 row_mask:0xf bank_mask:0xf
  v_add_f32_dpp v20, v20, v20 row_ror:2 row_mask:0xf bank_mask:0xf
  ds_write_b32 v24, v16
  ds_write_b32 v24, v20 offset:128
  s_waitcnt lgkmcnt(0)
  s_barrier
  s_cmp_lg_u32 s44, 0
  s_cbranch_scc1 Lup4_%=
  s_mov_b64 exec, 0xff
  v_lshlrev_b32 v24, 2, v1
  v_add_u32 v24, 0x27e00, v24
  s_waitcnt vmcnt(11)
  ds_read_b128 v[8:11], v24
  s_waitcnt vmcnt(10)
  ds_read_b128 v[12:15], v24 offset:16
  ds_read_b128 v[16:19], v24 offset:32
  ds_read_b128 v[20:23], v24 offset:48
  s_lshr_b32 s56, s3, 5
  s_bitcmp1_b32 18, s56
  s_cselect_b32 s55, 1.0, 0
  s_lshl_b32 s56, s3, 6
  s_add_u32 s56, s56, 0x1000
  s_waitcnt vmcnt(11) lgkmcnt(3)
  v_add_f32 v8, v8, v9
  s_waitcnt vmcnt(11)
  v_add_f32 v10, v10, v11
  s_waitcnt vmcnt(10) lgkmcnt(2)
  v_add_f32 v12, v12, v13
  s_waitcnt vmcnt(10)
  v_add_f32 v14, v14, v15
  s_waitcnt lgkmcnt(1)
  v_add_f32 v16, v16, v17
  v_add_f32 v18, v18, v19
  s_waitcnt lgkmcnt(0)
  v_add_f32 v20, v20, v21
  v_add_f32 v22, v22, v23
  s_waitcnt vmcnt(11)
  v_add_f32 v8, v8, v10
  s_waitcnt vmcnt(10)
  v_add_f32 v12, v12, v14
  v_add_f32 v16, v16, v18
  v_add_f32 v20, v20, v22
  s_waitcnt vmcnt(10)
  v_add_f32 v8, v8, v12
  v_add_f32 v16, v16, v20
  s_waitcnt vmcnt(11)
  v_cvt_pk_bf16_f32 v8, v8, v16
  s_waitcnt vmcnt(11)
  v_lshlrev_b32 v9, 16, v8
  s_waitcnt vmcnt(11)
  v_and_b32 v8, 0xffff0000, v8
  s_waitcnt vmcnt(11)
  v_add_f32 v9, s55, v9
  s_waitcnt vmcnt(11)
  v_add_f32 v8, s55, v8
  s_waitcnt vmcnt(11)
  v_cvt_pk_bf16_f32 v8, v9, v8
  s_waitcnt vmcnt(11)
  v_mov_b32 v9, s49
  s_waitcnt vmcnt(11)
  buffer_store_dwordx2 v[8:9], v5, s[36:39], s56 offen sc1
  s_mov_b64 exec, -1
Lup4_%=:
Lsk3_%=:
  s_mov_b32 s76, 0x3f803f80
  v_mov_b32 v28, -1
  s_waitcnt vmcnt(12)
  v_readfirstlane_b32 s49, v39
  s_add_u32 s49, s49, 1
  s_waitcnt vmcnt(8)
  v_dot2_f32_bf16 v8, v60, s76, 0
  v_dot2_f32_bf16 v14, v60, v60, 0
  v_dot2_f32_bf16 v8, v61, s76, v8
  v_dot2_f32_bf16 v14, v61, v61, v14
  v_dot2_f32_bf16 v8, v62, s76, v8
  v_dot2_f32_bf16 v14, v62, v62, v14
  v_mov_b32 v29, -1
  v_dot2_f32_bf16 v8, v63, s76, v8
  v_dot2_f32_bf16 v14, v63, v63, v14
  v_lshlrev_b32 v56, 16, v60
  v_and_b32 v57, 0xffff0000, v60
  v_lshlrev_b32 v58, 16, v61
  v_and_b32 v59, 0xffff0000, v61
  v_lshlrev_b32 v60, 16, v62
  v_mov_b32 v30, -1
  v_and_b32 v61, 0xffff0000, v62
  v_lshlrev_b32 v62, 16, v63
  v_and_b32 v63, 0xffff0000, v63
  s_waitcnt vmcnt(7)
  v_dot2_f32_bf16 v9, v68, s76, 0
  v_dot2_f32_bf16 v15, v68, v68, 0
  v_dot2_f32_bf16 v9, v69, s76, v9
  v_dot2_f32_bf16 v15, v69, v69, v15
  v_dot2_f32_bf16 v9, v70, s76, v9
  v_mov_b32 v31, -1
  v_dot2_f32_bf16 v15, v70, v70, v15
  v_dot2_f32_bf16 v9, v71, s76, v9
  v_dot2_f32_bf16 v15, v71, v71, v15
  v_lshlrev_b32 v64, 16, v68
  v_and_b32 v65, 0xffff0000, v68
  v_lshlrev_b32 v66, 16, v69
  v_and_b32 v67, 0xffff0000, v69
  s_lshr_b32 s55, s44, 1
  s_and_b32 s56, s44, 1
  s_lshl1_add_u32 s57, s48, s56
  s_xor_b32 s57, s57, s31
  s_lshl4_add_u32 s57, s55, s57
  s_lshl_b32 s58, s57, 10
  s_add_u32 s58, s58, 0x8000
  buffer_store_dwordx4 v[28:31], v1, s[40:43], s58 offen sc1
  v_lshlrev_b32 v68, 16, v70
  v_and_b32 v69, 0xffff0000, v70
  v_lshlrev_b32 v70, 16, v71
  v_and_b32 v71, 0xffff0000, v71
  s_waitcnt vmcnt(7)
  v_dot2_f32_bf16 v10, v76, s76, 0
  v_dot2_f32_bf16 v16, v76, v76, 0
  v_dot2_f32_bf16 v10, v77, s76, v10
  v_dot2_f32_bf16 v16, v77, v77, v16
  s_add_u32 m0, s51, 0x6000
  s_xor_b32 s59, s78, 1
  s_mul_i32 s59, s59, 0x30000
  s_add_u32 s59, s52, s59
  buffer_load_dwordx4 v4, s[8:11], s59 offen lds
  v_dot2_f32_bf16 v10, v78, s76, v10
  v_dot2_f32_bf16 v16, v78, v78, v16
  v_dot2_f32_bf16 v10, v79, s76, v10
  v_dot2_f32_bf16 v16, v79, v79, v16
  v_lshlrev_b32 v72, 16, v76
  v_and_b32 v73, 0xffff0000, v76
  v_lshlrev_b32 v74, 16, v77
  buffer_load_dwordx4 v4, s[8:11], s59 offen offset:1024 lds
  v_and_b32 v75, 0xffff0000, v77
  v_lshlrev_b32 v76, 16, v78
  v_and_b32 v77, 0xffff0000, v78
  v_lshlrev_b32 v78, 16, v79
  v_and_b32 v79, 0xffff0000, v79
  s_waitcnt vmcnt(8)
  v_dot2_f32_bf16 v11, v84, s76, 0
  v_dot2_f32_bf16 v17, v84, v84, 0
  buffer_load_dwordx4 v4, s[8:11], s59 offen offset:2048 lds
  v_dot2_f32_bf16 v11, v85, s76, v11
  v_dot2_f32_bf16 v17, v85, v85, v17
  v_dot2_f32_bf16 v11, v86, s76, v11
  v_dot2_f32_bf16 v17, v86, v86, v17
  v_dot2_f32_bf16 v11, v87, s76, v11
  v_dot2_f32_bf16 v17, v87, v87, v17
  v_lshlrev_b32 v80, 16, v84
  v_and_b32 v81, 0xffff0000, v84
  buffer_load_dwordx4 v[108:111], v6, s[4:7], s75 offen
  v_lshlrev_b32 v82, 16, v85
  v_and_b32 v83, 0xffff0000, v85
  v_lshlrev_b32 v84, 16, v86
  v_and_b32 v85, 0xffff0000, v86
  v_lshlrev_b32 v86, 16, v87
  v_and_b32 v87, 0xffff0000, v87
  s_waitcnt vmcnt(9)
  v_dot2_f32_bf16 v12, v92, s76, 0
  buffer_load_dwordx4 v[116:119], v6, s[4:7], s75 offen offset:2048
  v_dot2_f32_bf16 v18, v92, v92, 0
  v_dot2_f32_bf16 v12, v93, s76, v12
  v_dot2_f32_bf16 v18, v93, v93, v18
  v_dot2_f32_bf16 v12, v94, s76, v12
  v_dot2_f32_bf16 v18, v94, v94, v18
  v_dot2_f32_bf16 v12, v95, s76, v12
  v_dot2_f32_bf16 v18, v95, v95, v18
  v_lshlrev_b32 v88, 16, v92
  buffer_load_dwordx4 v[124:127], v6, s[4:7], s75 offen offset:1024
  v_and_b32 v89, 0xffff0000, v92
  v_lshlrev_b32 v90, 16, v93
  v_and_b32 v91, 0xffff0000, v93
  v_lshlrev_b32 v92, 16, v94
  v_and_b32 v93, 0xffff0000, v94
  v_lshlrev_b32 v94, 16, v95
  v_and_b32 v95, 0xffff0000, v95
  buffer_load_dwordx4 v[132:135], v6, s[4:7], s75 offen offset:3072
  s_waitcnt vmcnt(11)
  v_dot2_f32_bf16 v13, v100, s76, 0
  v_dot2_f32_bf16 v19, v100, v100, 0
  v_dot2_f32_bf16 v13, v101, s76, v13
  v_dot2_f32_bf16 v19, v101, v101, v19
  v_dot2_f32_bf16 v13, v102, s76, v13
  v_dot2_f32_bf16 v19, v102, v102, v19
  v_dot2_f32_bf16 v13, v103, s76, v13
  s_add_u32 m0, s51, 0x14500
  s_xor_b32 s59, s78, 2
  s_mul_i32 s59, s59, 0x30000
  s_add_u32 s59, s52, s59
  buffer_load_dwordx4 v4, s[8:11], s59 offen lds
  v_dot2_f32_bf16 v19, v103, v103, v19
  v_lshlrev_b32 v96, 16, v100
  v_and_b32 v97, 0xffff0000, v100
  v_lshlrev_b32 v98, 16, v101
  v_and_b32 v99, 0xffff0000, v101
  v_lshlrev_b32 v100, 16, v102
  v_and_b32 v101, 0xffff0000, v102
  v_lshlrev_b32 v102, 16, v103
  buffer_load_dwordx4 v4, s[8:11], s59 offen offset:1024 lds
  v_and_b32 v103, 0xffff0000, v103
  v_permlane32_swap_b32 v8, v10
  v_permlane32_swap_b32 v9, v11
  v_add_f32 v8, v8, v10
  v_add_f32 v9, v9, v11
  s_nop 1
  v_permlane16_swap_b32 v8, v9
  v_add_f32 v8, v8, v9
  buffer_load_dwordx4 v4, s[8:11], s59 offen offset:2048 lds
  v_permlane32_swap_b32 v12, v13
  v_add_f32 v12, v12, v13
  v_mov_b32 v13, v12
  s_nop 1
  v_permlane16_swap_b32 v12, v13
  v_add_f32 v12, v12, v13
  v_permlane32_swap_b32 v14, v16
  v_permlane32_swap_b32 v15, v17
  v_add_f32 v14, v14, v16
  v_add_f32 v15, v15, v17
  s_nop 1
  v_permlane16_swap_b32 v14, v15
  v_add_f32 v14, v14, v15
  v_permlane32_swap_b32 v18, v19
  v_add_f32 v18, v18, v19
  v_mov_b32 v19, v18
  s_nop 1
  v_permlane16_swap_b32 v18, v19
  v_add_f32 v18, v18, v19
  v_add_f32_dpp v8, v8, v8 row_ror:8 row_mask:0xf bank_mask:0xf
  v_add_f32_dpp v12, v12, v12 row_ror:8 row_mask:0xf bank_mask:0xf
  v_add_f32_dpp v14, v14, v14 row_ror:8 row_mask:0xf bank_mask:0xf
  v_add_f32_dpp v18, v18, v18 row_ror:8 row_mask:0xf bank_mask:0xf
  v_add_f32_dpp v8, v8, v8 row_ror:4 row_mask:0xf bank_mask:0xf
  v_add_f32_dpp v12, v12, v12 row_ror:4 row_mask:0xf bank_mask:0xf
  v_add_f32_dpp v14, v14, v14 row_ror:4 row_mask:0xf bank_mask:0xf
  v_add_f32_dpp v18, v18, v18 row_ror:4 row_mask:0xf bank_mask:0xf
  v_add_f32_dpp v8, v8, v8 row_ror:2 row_mask:0xf bank_mask:0xf
  v_add_f32_dpp v12, v12, v12 row_ror:2 row_mask:0xf bank_mask:0xf
  v_add_f32_dpp v14, v14, v14 row_ror:2 row_mask:0xf bank_mask:0xf
  v_add_f32_dpp v18, v18, v18 row_ror:2 row_mask:0xf bank_mask:0xf
  v_add_f32_dpp v8, v8, v8 row_ror:1 row_mask:0xf bank_mask:0xf
  v_add_f32_dpp v12, v12, v12 row_ror:1 row_mask:0xf bank_mask:0xf
  v_add_f32_dpp v14, v14, v14 row_ror:1 row_mask:0xf bank_mask:0xf
  v_add_f32_dpp v18, v18, v18 row_ror:1 row_mask:0xf bank_mask:0xf
  v_mul_f32 v8, 0xbb000000, v8
  v_mul_f32 v14, 0x3b000000, v14
  v_fma_f32 v14, -v8, v8, v14
  v_max_f32 v14, 0, v14
  v_add_f32 v14, 0x3727c5ac, v14
  v_rsq_f32 v14, v14
  v_mul_f32 v12, 0xbb000000, v12
  v_mul_f32 v18, 0x3b000000, v18
  v_fma_f32 v18, -v12, v12, v18
  v_max_f32 v18, 0, v18
  v_add_f32 v18, 0x3727c5ac, v18
  v_rsq_f32 v18, v18
  v_mul_f32 v8, v8, v14
  v_mul_f32 v12, v12, v18
  v_readlane_b32 s60, v14, 0
  v_readlane_b32 s61, v8, 0
  v_readlane_b32 s62, v14, 16
  v_readlane_b32 s63, v8, 16
  v_readlane_b32 s64, v14, 32
  v_readlane_b32 s65, v8, 32
  v_readlane_b32 s66, v14, 48
  v_readlane_b32 s67, v8, 48
  v_readlane_b32 s68, v18, 0
  v_readlane_b32 s69, v12, 0
  v_readlane_b32 s70, v18, 32
  v_readlane_b32 s71, v12, 32
  v_pk_fma_f32 v[56:57], v[56:57], s[60:61], s[60:61] op_sel:[0,0,1] op_sel_hi:[1,0,1]
  v_pk_fma_f32 v[58:59], v[58:59], s[60:61], s[60:61] op_sel:[0,0,1] op_sel_hi:[1,0,1]
  v_pk_fma_f32 v[60:61], v[60:61], s[60:61], s[60:61] op_sel:[0,0,1] op_sel_hi:[1,0,1]
  v_pk_fma_f32 v[62:63], v[62:63], s[60:61], s[60:61] op_sel:[0,0,1] op_sel_hi:[1,0,1]
  v_pk_fma_f32 v[64:65], v[64:65], s[62:63], s[62:63] op_sel:[0,0,1] op_sel_hi:[1,0,1]
  v_pk_fma_f32 v[66:67], v[66:67], s[62:63], s[62:63] op_sel:[0,0,1] op_sel_hi:[1,0,1]
  v_pk_fma_f32 v[68:69], v[68:69], s[62:63], s[62:63] op_sel:[0,0,1] op_sel_hi:[1,0,1]
  v_pk_fma_f32 v[70:71], v[70:71], s[62:63], s[62:63] op_sel:[0,0,1] op_sel_hi:[1,0,1]
  v_pk_fma_f32 v[72:73], v[72:73], s[64:65], s[64:65] op_sel:[0,0,1] op_sel_hi:[1,0,1]
  v_pk_fma_f32 v[74:75], v[74:75], s[64:65], s[64:65] op_sel:[0,0,1] op_sel_hi:[1,0,1]
  v_pk_fma_f32 v[76:77], v[76:77], s[64:65], s[64:65] op_sel:[0,0,1] op_sel_hi:[1,0,1]
  v_pk_fma_f32 v[78:79], v[78:79], s[64:65], s[64:65] op_sel:[0,0,1] op_sel_hi:[1,0,1]
  v_pk_fma_f32 v[80:81], v[80:81], s[66:67], s[66:67] op_sel:[0,0,1] op_sel_hi:[1,0,1]
  v_pk_fma_f32 v[82:83], v[82:83], s[66:67], s[66:67] op_sel:[0,0,1] op_sel_hi:[1,0,1]
  v_pk_fma_f32 v[84:85], v[84:85], s[66:67], s[66:67] op_sel:[0,0,1] op_sel_hi:[1,0,1]
  v_pk_fma_f32 v[86:87], v[86:87], s[66:67], s[66:67] op_sel:[0,0,1] op_sel_hi:[1,0,1]
  v_pk_fma_f32 v[88:89], v[88:89], s[68:69], s[68:69] op_sel:[0,0,1] op_sel_hi:[1,0,1]
  v_pk_fma_f32 v[90:91], v[90:91], s[68:69], s[68:69] op_sel:[0,0,1] op_sel_hi:[1,0,1]
  v_pk_fma_f32 v[92:93], v[92:93], s[68:69], s[68:69] op_sel:[0,0,1] op_sel_hi:[1,0,1]
  v_pk_fma_f32 v[94:95], v[94:95], s[68:69], s[68:69] op_sel:[0,0,1] op_sel_hi:[1,0,1]
  v_pk_fma_f32 v[96:97], v[96:97], s[70:71], s[70:71] op_sel:[0,0,1] op_sel_hi:[1,0,1]
  v_pk_fma_f32 v[98:99], v[98:99], s[70:71], s[70:71] op_sel:[0,0,1] op_sel_hi:[1,0,1]
  v_pk_fma_f32 v[100:101], v[100:101], s[70:71], s[70:71] op_sel:[0,0,1] op_sel_hi:[1,0,1]
  v_pk_fma_f32 v[102:103], v[102:103], s[70:71], s[70:71] op_sel:[0,0,1] op_sel_hi:[1,0,1]
  v_mov_b32 v12, s49
  s_lshl_b32 s56, s48, 6
  s_cmp_eq_u32 s44, 1
  s_cselect_b32 s56, s56, 0x40000000
  s_waitcnt vmcnt(7)
  s_barrier
  s_mov_b64 exec, 1
  buffer_store_dword v12, off, s[40:43], s56 sc1
  s_mov_b64 exec, -1
  s_add_u32 m0, s51, 0x1a500
  s_xor_b32 s59, s78, 3
  s_mul_i32 s59, s59, 0x30000
  s_add_u32 s59, s52, s59
  buffer_load_dwordx4 v4, s[8:11], s59 offen lds
  ds_read_b64_tr_b16 v[136:137], v52
  s_waitcnt vmcnt(8)
  v_dot2_f32_bf16 v8, v108, s76, 0
  ds_read_b64_tr_b16 v[138:139], v52 offset:6144
  v_dot2_f32_bf16 v12, v108, v108, 0
  v_dot2_f32_bf16 v8, v109, s76, v8
  ds_read_b64_tr_b16 v[140:141], v52 offset:1024
  v_dot2_f32_bf16 v12, v109, v109, v12
  v_dot2_f32_bf16 v8, v110, s76, v8
  ds_read_b64_tr_b16 v[142:143], v52 offset:7168
  v_dot2_f32_bf16 v12, v110, v110, v12
  buffer_load_dwordx4 v4, s[8:11], s59 offen offset:1024 lds
  ds_read_b64_tr_b16 v[144:145], v52 offset:2048
  v_dot2_f32_bf16 v8, v111, s76, v8
  v_dot2_f32_bf16 v12, v111, v111, v12
  ds_read_b64_tr_b16 v[146:147], v52 offset:8192
  v_lshlrev_b32 v104, 16, v108
  v_and_b32 v105, 0xffff0000, v108
  ds_read_b64_tr_b16 v[148:149], v52 offset:12288
  v_lshlrev_b32 v106, 16, v109
  ds_read_b64_tr_b16 v[150:151], v52 offset:18432
  v_and_b32 v107, 0xffff0000, v109
  v_lshlrev_b32 v108, 16, v110
  buffer_load_dwordx4 v4, s[8:11], s59 offen offset:2048 lds
  ds_read_b64_tr_b16 v[152:153], v52 offset:13312
  v_and_b32 v109, 0xffff0000, v110
  v_lshlrev_b32 v110, 16, v111
  ds_read_b64_tr_b16 v[154:155], v52 offset:19456
  v_and_b32 v111, 0xffff0000, v111
  s_waitcnt vmcnt(9)
  v_dot2_f32_bf16 v9, v116, s76, 0
  ds_read_b64_tr_b16 v[156:157], v52 offset:14336
  v_dot2_f32_bf16 v13, v116, v116, 0
  ds_read_b64_tr_b16 v[158:159], v52 offset:20480
  v_dot2_f32_bf16 v9, v117, s76, v9
  v_dot2_f32_bf16 v13, v117, v117, v13
  buffer_load_dwordx2 v[20:21], v5, s[36:39], s72 offen sc1
  ds_read_b64_tr_b16 v[160:161], v52 offset:24576
  v_dot2_f32_bf16 v9, v118, s76, v9
  v_dot2_f32_bf16 v13, v118, v118, v13
  ds_read_b64_tr_b16 v[162:163], v52 offset:30720
  v_dot2_f32_bf16 v9, v119, s76, v9
  ds_read_b64_tr_b16 v[164:165], v52 offset:25600
  v_dot2_f32_bf16 v13, v119, v119, v13
  v_lshlrev_b32 v112, 16, v116
  ds_read_b64_tr_b16 v[166:167], v52 offset:31744
  v_and_b32 v113, 0xffff0000, v116
  v_lshlrev_b32 v114, 16, v117
  buffer_load_dwordx2 v[24:25], v5, s[36:39], s73 offen sc1
  ds_read_b64_tr_b16 v[168:169], v52 offset:26624
  v_and_b32 v115, 0xffff0000, v117
  v_lshlrev_b32 v116, 16, v118
  ds_read_b64_tr_b16 v[170:171], v52 offset:32768
  v_and_b32 v117, 0xffff0000, v118
  ds_read_b64_tr_b16 v[172:173], v52 offset:36864
  v_lshlrev_b32 v118, 16, v119
  v_and_b32 v119, 0xffff0000, v119
  ds_read_b64_tr_b16 v[174:175], v52 offset:43008
  s_waitcnt vmcnt(10)
  v_dot2_f32_bf16 v10, v124, s76, 0
  buffer_load_dwordx2 v[26:27], v240, s[36:39], s74 offen sc1
  v_dot2_f32_bf16 v14, v124, v124, 0
  ds_read_b64_tr_b16 v[176:177], v52 offset:37888
  v_dot2_f32_bf16 v10, v125, s76, v10
  ds_read_b64_tr_b16 v[178:179], v52 offset:44032
  v_dot2_f32_bf16 v14, v125, v125, v14
  v_dot2_f32_bf16 v10, v126, s76, v10
  ds_read_b64_tr_b16 v[180:181], v52 offset:38912
  v_dot2_f32_bf16 v14, v126, v126, v14
  v_dot2_f32_bf16 v10, v127, s76, v10
  ds_read_b64_tr_b16 v[182:183], v52 offset:45056
  v_dot2_f32_bf16 v14, v127, v127, v14
  s_add_u32 m0, s51, 0x20500
  s_xor_b32 s59, s78, 4
  s_mul_i32 s59, s59, 0x30000
  s_add_u32 s59, s52, s59
  buffer_load_dwordx4 v4, s[8:11], s59 offen lds
  v_lshlrev_b32 v120, 16, v124
  v_and_b32 v121, 0xffff0000, v124
  v_lshlrev_b32 v122, 16, v125
  v_and_b32 v123, 0xffff0000, v125
  v_lshlrev_b32 v124, 16, v126
  v_and_b32 v125, 0xffff0000, v126
  v_lshlrev_b32 v126, 16, v127
  buffer_load_dwordx4 v4, s[8:11], s59 offen offset:1024 lds
  v_and_b32 v127, 0xffff0000, v127
  s_waitcnt vmcnt(12)
  v_dot2_f32_bf16 v11, v132, s76, 0
  v_dot2_f32_bf16 v15, v132, v132, 0
  v_dot2_f32_bf16 v11, v133, s76, v11
  v_dot2_f32_bf16 v15, v133, v133, v15
  v_dot2_f32_bf16 v11, v134, s76, v11
  v_dot2_f32_bf16 v15, v134, v134, v15
  buffer_load_dwordx4 v4, s[8:11], s59 offen offset:2048 lds
  v_dot2_f32_bf16 v11, v135, s76, v11
  v_dot2_f32_bf16 v15, v135, v135, v15
  v_lshlrev_b32 v128, 16, v132
  v_and_b32 v129, 0xffff0000, v132
  v_lshlrev_b32 v130, 16, v133
  v_and_b32 v131, 0xffff0000, v133
  s_add_u32 m0, s51, 0xc300
  s_xor_b32 s59, s78, 5
  s_mul_i32 s59, s59, 0x30000
  s_add_u32 s59, s52, s59
  buffer_load_dwordx4 v4, s[8:11], s59 offen lds
  v_lshlrev_b32 v132, 16, v134
  v_and_b32 v133, 0xffff0000, v134
  v_lshlrev_b32 v134, 16, v135
  v_and_b32 v135, 0xffff0000, v135
  v_permlane32_swap_b32 v8, v10
  v_permlane32_swap_b32 v9, v11
  v_add_f32 v8, v8, v10
  buffer_load_dwordx4 v4, s[8:11], s59 offen offset:1024 lds
  v_add_f32 v9, v9, v11
  s_nop 1
  v_permlane16_swap_b32 v8, v9
  v_add_f32 v8, v8, v9
  v_permlane32_swap_b32 v12, v14
  v_permlane32_swap_b32 v13, v15
  v_add_f32 v12, v12, v14
  v_add_f32 v13, v13, v15
  buffer_load_dwordx4 v4, s[8:11], s59 offen offset:2048 lds
  s_nop 0
  v_permlane16_swap_b32 v12, v13
  v_add_f32 v12, v12, v13
  v_add_f32_dpp v8, v8, v8 row_ror:8 row_mask:0xf bank_mask:0xf
  s_nop 0
  v_add_f32_dpp v12, v12, v12 row_ror:8 row_mask:0xf bank_mask:0xf
  v_add_f32_dpp v8, v8, v8 row_ror:4 row_mask:0xf bank_mask:0xf
  s_nop 0
  v_add_f32_dpp v12, v12, v12 row_ror:4 row_mask:0xf bank_mask:0xf
  v_add_f32_dpp v8, v8, v8 row_ror:2 row_mask:0xf bank_mask:0xf
  s_nop 0
  v_add_f32_dpp v12, v12, v12 row_ror:2 row_mask:0xf bank_mask:0xf
  v_add_f32_dpp v8, v8, v8 row_ror:1 row_mask:0xf bank_mask:0xf
  s_nop 0
  v_add_f32_dpp v12, v12, v12 row_ror:1 row_mask:0xf bank_mask:0xf
  v_mul_f32 v8, 0xbb000000, v8
  v_mul_f32 v12, 0x3b000000, v12
  v_fma_f32 v12, -v8, v8, v12
  v_max_f32 v12, 0, v12
  v_add_f32 v12, 0x3727c5ac, v12
  v_rsq_f32 v12, v12
  s_nop 0
  v_mul_f32 v8, v8, v12
  v_readlane_b32 s60, v12, 0
  v_readlane_b32 s61, v8, 0
  v_readlane_b32 s62, v12, 16
  v_readlane_b32 s63, v8, 16
  v_readlane_b32 s64, v12, 32
  v_readlane_b32 s65, v8, 32
  v_readlane_b32 s66, v12, 48
  v_readlane_b32 s67, v8, 48
  v_pk_fma_f32 v[104:105], v[104:105], s[60:61], s[60:61] op_sel:[0,0,1] op_sel_hi:[1,0,1]
  v_pk_fma_f32 v[106:107], v[106:107], s[60:61], s[60:61] op_sel:[0,0,1] op_sel_hi:[1,0,1]
  v_pk_fma_f32 v[108:109], v[108:109], s[60:61], s[60:61] op_sel:[0,0,1] op_sel_hi:[1,0,1]
  v_pk_fma_f32 v[110:111], v[110:111], s[60:61], s[60:61] op_sel:[0,0,1] op_sel_hi:[1,0,1]
  v_pk_fma_f32 v[112:113], v[112:113], s[62:63], s[62:63] op_sel:[0,0,1] op_sel_hi:[1,0,1]
  v_pk_fma_f32 v[114:115], v[114:115], s[62:63], s[62:63] op_sel:[0,0,1] op_sel_hi:[1,0,1]
  v_pk_fma_f32 v[116:117], v[116:117], s[62:63], s[62:63] op_sel:[0,0,1] op_sel_hi:[1,0,1]
  v_pk_fma_f32 v[118:119], v[118:119], s[62:63], s[62:63] op_sel:[0,0,1] op_sel_hi:[1,0,1]
  v_pk_fma_f32 v[120:121], v[120:121], s[64:65], s[64:65] op_sel:[0,0,1] op_sel_hi:[1,0,1]
  v_pk_fma_f32 v[122:123], v[122:123], s[64:65], s[64:65] op_sel:[0,0,1] op_sel_hi:[1,0,1]
  v_pk_fma_f32 v[124:125], v[124:125], s[64:65], s[64:65] op_sel:[0,0,1] op_sel_hi:[1,0,1]
  v_pk_fma_f32 v[126:127], v[126:127], s[64:65], s[64:65] op_sel:[0,0,1] op_sel_hi:[1,0,1]
  v_pk_fma_f32 v[128:129], v[128:129], s[66:67], s[66:67] op_sel:[0,0,1] op_sel_hi:[1,0,1]
  v_pk_fma_f32 v[130:131], v[130:131], s[66:67], s[66:67] op_sel:[0,0,1] op_sel_hi:[1,0,1]
  v_pk_fma_f32 v[132:133], v[132:133], s[66:67], s[66:67] op_sel:[0,0,1] op_sel_hi:[1,0,1]
  v_pk_fma_f32 v[134:135], v[134:135], s[66:67], s[66:67] op_sel:[0,0,1] op_sel_hi:[1,0,1]
Lck5_%=:
  s_waitcnt vmcnt(8)
  v_cmp_ne_u32 vcc, s49, v21
  s_waitcnt vmcnt(7)
  v_cmp_ne_u32 s[58:59], s49, v25
  s_or_b64 vcc, vcc, s[58:59]
  s_waitcnt vmcnt(6)
  v_cmp_ne_u32 s[58:59], s49, v27
  s_or_b64 vcc, vcc, s[58:59]
  s_and_b64 vcc, vcc, s[0:1]
  s_cbranch_scc1 Lsl6_%=
  v_lshlrev_b32 v14, 16, v20
  v_and_b32 v15, 0xffff0000, v20
  ds_write_b64 v238, v[14:15]
  ds_write_b32 v241, v24
  ds_write_b32 v242, v26
  s_waitcnt lgkmcnt(0)
  s_barrier
  ds_read_b128 v[44:47], v239 offset:2048
  ds_read_b128 v[36:39], v239
  ds_read_b128 v[48:51], v239 offset:2064
  ds_read_b128 v[40:43], v239 offset:16
  s_mul_i32 s77, s44, 0x820
  v_mov_b32 v6, v233
  v_mov_b32 v5, v232
  s_add_u32 m0, s77, 0
  ds_read_b64_tr_b16 v[184:185], v53 offset:21760
  s_waitcnt lgkmcnt(3)
  v_pk_fma_f32 v[56:57], v[56:57], v[44:45], v[36:37]
  ds_read_b64_tr_b16 v[186:187], v53 offset:27904
  v_pk_fma_f32 v[58:59], v[58:59], v[46:47], v[38:39]
  ds_read_b64_tr_b16 v[188:189], v53 offset:22784
  s_waitcnt lgkmcnt(3)
  v_pk_fma_f32 v[60:61], v[60:61], v[48:49], v[40:41]
  ds_read_b64_tr_b16 v[190:191], v53 offset:28928
  v_pk_fma_f32 v[62:63], v[62:63], v[50:51], v[42:43]
  v_cvt_pk_bf16_f32 v56, v56, v57
  ds_read_b64_tr_b16 v[192:193], v53 offset:23808
  v_cvt_pk_bf16_f32 v58, v58, v59
  ds_read_b64_tr_b16 v[194:195], v53 offset:29952
  v_cvt_pk_bf16_f32 v60, v60, v61
  ds_read_b64_tr_b16 v[196:197], v53 offset:34048
  v_cvt_pk_bf16_f32 v62, v62, v63
  s_add_u32 s57, s97, 0x18000
  buffer_store_dwordx4 v[28:31], v234, s[40:43], s57 offen sc1
  ds_write_addtid_b32 v56 offset:0
  ds_write_addtid_b32 v58 offset:256
  ds_write_addtid_b32 v60 offset:512
  ds_write_addtid_b32 v62 offset:768
  v_pk_fma_f32 v[64:65], v[64:65], v[44:45], v[36:37]
  ds_read_b64_tr_b16 v[198:199], v53 offset:40192
  v_pk_fma_f32 v[66:67], v[66:67], v[46:47], v[38:39]
  ds_read_b64_tr_b16 v[200:201], v53 offset:35072
  v_pk_fma_f32 v[68:69], v[68:69], v[48:49], v[40:41]
  ds_read_b64_tr_b16 v[202:203], v53 offset:41216
  v_pk_fma_f32 v[70:71], v[70:71], v[50:51], v[42:43]
  ds_read_b64_tr_b16 v[204:205], v53 offset:36096
  v_cvt_pk_bf16_f32 v64, v64, v65
  v_cvt_pk_bf16_f32 v66, v66, v67
  ds_read_b64_tr_b16 v[206:207], v53 offset:42240
  v_cvt_pk_bf16_f32 v68, v68, v69
  ds_read_b64_tr_b16 v[208:209], v53 offset:46336
  v_cvt_pk_bf16_f32 v70, v70, v71
  ds_read_b64_tr_b16 v[210:211], v53 offset:52480
  buffer_store_dwordx4 v[28:31], v6, s[40:43], s24 offen sc1
  ds_write_addtid_b32 v64 offset:1040
  ds_write_addtid_b32 v66 offset:1296
  ds_write_addtid_b32 v68 offset:1552
  ds_write_addtid_b32 v70 offset:1808
  v_pk_fma_f32 v[72:73], v[72:73], v[44:45], v[36:37]
  v_pk_fma_f32 v[74:75], v[74:75], v[46:47], v[38:39]
  ds_read_b64_tr_b16 v[212:213], v53 offset:47360
  v_pk_fma_f32 v[76:77], v[76:77], v[48:49], v[40:41]
  ds_read_b64_tr_b16 v[214:215], v53 offset:53504
  v_pk_fma_f32 v[78:79], v[78:79], v[50:51], v[42:43]
  ds_read_b64_tr_b16 v[216:217], v53 offset:48384
  v_cvt_pk_bf16_f32 v72, v72, v73
  ds_read_b64_tr_b16 v[218:219], v53 offset:54528
  v_cvt_pk_bf16_f32 v74, v74, v75
  v_cvt_pk_bf16_f32 v76, v76, v77
  ds_read_b64_tr_b16 v[220:221], v53 offset:58624
  v_cvt_pk_bf16_f32 v78, v78, v79
  ds_read_b64_tr_b16 v[222:223], v54 offset:3328
  buffer_store_dwordx4 v[28:31], v6, s[40:43], s25 offen sc1
  ds_write_addtid_b32 v72 offset:16640
  ds_write_addtid_b32 v74 offset:16896
  ds_write_addtid_b32 v76 offset:17152
  ds_write_addtid_b32 v78 offset:17408
  v_pk_fma_f32 v[80:81], v[80:81], v[44:45], v[36:37]
  ds_read_b64_tr_b16 v[224:225], v53 offset:59648
  v_pk_fma_f32 v[82:83], v[82:83], v[46:47], v[38:39]
  v_pk_fma_f32 v[84:85], v[84:85], v[48:49], v[40:41]
  ds_read_b64_tr_b16 v[226:227], v54 offset:4352
  v_pk_fma_f32 v[86:87], v[86:87], v[50:51], v[42:43]
  ds_read_b64_tr_b16 v[228:229], v53 offset:60672
  v_cvt_pk_bf16_f32 v80, v80, v81
  ds_read_b64_tr_b16 v[230:231], v54 offset:5376
  v_cvt_pk_bf16_f32 v82, v82, v83
  v_cvt_pk_bf16_f32 v84, v84, v85
  v_cvt_pk_bf16_f32 v86, v86, v87
  buffer_store_dwordx4 v[28:31], v6, s[40:43], s26 offen sc1
  ds_write_addtid_b32 v80 offset:17680
  ds_write_addtid_b32 v82 offset:17936
  ds_write_addtid_b32 v84 offset:18192
  ds_write_addtid_b32 v86 offset:18448
  v_pk_fma_f32 v[88:89], v[88:89], v[44:45], v[36:37]
  v_pk_fma_f32 v[90:91], v[90:91], v[46:47], v[38:39]
  v_pk_fma_f32 v[92:93], v[92:93], v[48:49], v[40:41]
  v_pk_fma_f32 v[94:95], v[94:95], v[50:51], v[42:43]
  v_cvt_pk_bf16_f32 v88, v88, v89
  v_cvt_pk_bf16_f32 v90, v90, v91
  v_cvt_pk_bf16_f32 v92, v92, v93
  v_cvt_pk_bf16_f32 v94, v94, v95
  s_lshr_b32 s57, s97, 2
  s_add_u32 s57, s57, 0x1000
  buffer_store_dword v28, v235, s[40:43], s57 offen sc1
  ds_write_addtid_b32 v88 offset:33280
  ds_write_addtid_b32 v90 offset:33536
  ds_write_addtid_b32 v92 offset:33792
  ds_write_addtid_b32 v94 offset:34048
  v_pk_fma_f32 v[96:97], v[96:97], v[44:45], v[36:37]
  v_pk_fma_f32 v[98:99], v[98:99], v[46:47], v[38:39]
  v_pk_fma_f32 v[100:101], v[100:101], v[48:49], v[40:41]
  v_pk_fma_f32 v[102:103], v[102:103], v[50:51], v[42:43]
  v_cvt_pk_bf16_f32 v96, v96, v97
  v_cvt_pk_bf16_f32 v98, v98, v99
  v_cvt_pk_bf16_f32 v100, v100, v101
  v_cvt_pk_bf16_f32 v102, v102, v103
  ds_write_addtid_b32 v96 offset:34320
  ds_write_addtid_b32 v98 offset:34576
  ds_write_addtid_b32 v100 offset:34832
  ds_write_addtid_b32 v102 offset:35088
  s_waitcnt vmcnt(5)
  s_waitcnt lgkmcnt(0)
  s_barrier
  ds_read_b128 v[56:59], v2
  ds_read_b128 v[28:31], v3
  ds_read_b128 v[60:63], v2 offset:16
  ds_read_b128 v[32:35], v3 offset:16
  ds_read_b64_tr_b16 v[232:233], v54 offset:9472
  ds_read_b64_tr_b16 v[234:235], v54 offset:15616
  s_add_u32 m0, s51, 0x14500
  s_xor_b32 s59, s78, 6
  s_mul_i32 s59, s59, 0x30000
  s_add_u32 s59, s52, s59
  buffer_load_dwordx4 v4, s[8:11], s59 offen lds
  s_waitcnt lgkmcnt(5)
  v_mfma_f32_16x16x32_bf16 v[8:11], v[136:139], v[56:59], 0
  ds_read_b64_tr_b16 v[236:237], v54 offset:10496
  ds_read_b64_tr_b16 v[238:239], v54 offset:16640
  ds_read_b128 v[96:99], v2 offset:32
  ds_read_b128 v[100:103], v3 offset:32
  v_mfma_f32_16x16x32_bf16 v[12:15], v[140:143], v[56:59], 0
  ds_read_b64_tr_b16 v[240:241], v54 offset:11520
  ds_read_b64_tr_b16 v[242:243], v54 offset:17664
  buffer_load_dwordx4 v4, s[8:11], s59 offen offset:1024 lds
  v_mfma_f32_16x16x32_bf16 v[16:19], v[56:59], v[144:147], 0
  ds_read_b64_tr_b16 v[244:245], v54 offset:21760
  ds_read_b64_tr_b16 v[246:247], v54 offset:27904
  s_waitcnt lgkmcnt(12)
  v_mfma_f32_16x16x32_bf16 v[20:23], v[140:143], v[28:31], 0
  ds_read_b64_tr_b16 v[248:249], v54 offset:22784
  ds_read_b64_tr_b16 v[250:251], v54 offset:28928
  v_mfma_f32_16x16x32_bf16 v[24:27], v[28:31], v[144:147], 0
  ds_read_b64_tr_b16 v[252:253], v54 offset:23808
  ds_read_b64_tr_b16 v[254:255], v54 offset:29952
  buffer_load_dwordx4 v4, s[8:11], s59 offen offset:2048 lds
  s_waitcnt lgkmcnt(15)
  v_mfma_f32_16x16x32_bf16 v[8:11], v[148:151], v[60:63], v[8:11]
  ds_read_b64_tr_b16 v[64:65], v52 offset:49920
  ds_read_b64_tr_b16 v[66:67], v52 offset:56064
  ds_read_b128 v[56:59], v2 offset:48
  ds_read_b128 v[28:31], v3 offset:48
  v_mfma_f32_16x16x32_bf16 v[12:15], v[152:155], v[60:63], v[12:15]
  ds_read_b64_tr_b16 v[68:69], v52 offset:50944
  ds_read_b64_tr_b16 v[70:71], v52 offset:57088
  s_add_u32 m0, s51, 0x1a500
  s_xor_b32 s59, s78, 7
  s_mul_i32 s59, s59, 0x30000
  s_add_u32 s59, s52, s59
  buffer_load_dwordx4 v4, s[8:11], s59 offen lds
  v_mfma_f32_16x16x32_bf16 v[16:19], v[60:63], v[156:159], v[16:19]
  ds_read_b64_tr_b16 v[72:73], v52 offset:51968
  ds_read_b64_tr_b16 v[74:75], v52 offset:58112
  s_waitcnt lgkmcnt(15)
  v_mfma_f32_16x16x32_bf16 v[20:23], v[152:155], v[32:35], v[20:23]
  ds_read_b64_tr_b16 v[76:77], v53 offset:768
  ds_read_b64_tr_b16 v[78:79], v53 offset:6912
  v_mfma_f32_16x16x32_bf16 v[24:27], v[32:35], v[156:159], v[24:27]
  ds_read_b64_tr_b16 v[80:81], v53 offset:1792
  ds_read_b64_tr_b16 v[82:83], v53 offset:7936
  buffer_load_dwordx4 v4, s[8:11], s59 offen offset:1024 lds
  s_waitcnt lgkmcnt(15)
  v_mfma_f32_16x16x32_bf16 v[8:11], v[160:163], v[96:99], v[8:11]
  ds_read_b64_tr_b16 v[84:85], v53 offset:2816
  ds_read_b64_tr_b16 v[86:87], v53 offset:8960
  ds_read_b128 v[60:63], v2 offset:64
  ds_read_b128 v[32:35], v3 offset:64
  v_mfma_f32_16x16x32_bf16 v[12:15], v[164:167], v[96:99], v[12:15]
  buffer_load_dwordx4 v4, s[8:11], s59 offen offset:2048 lds
  v_mfma_f32_16x16x32_bf16 v[16:19], v[96:99], v[168:171], v[16:19]
  s_waitcnt lgkmcnt(15)
  v_mfma_f32_16x16x32_bf16 v[20:23], v[164:167], v[100:103], v[20:23]
  v_mfma_f32_16x16x32_bf16 v[24:27], v[100:103], v[168:171], v[24:27]
  s_waitcnt lgkmcnt(0)
  s_barrier
  s_add_u32 m0, s96, 0x20500
  s_xor_b32 s59, s95, 0x18000
  buffer_load_dwordx4 v5, s[12:15], s59 offen lds
  s_add_u32 m0, s96, 0x20900
  s_xor_b32 s59, s95, 0x1c000
  buffer_load_dwordx4 v5, s[12:15], s59 offen lds
  s_add_u32 m0, s77, 0x8200
  v_fma_f32 v104, v104, v44, v36
  v_mfma_f32_16x16x32_bf16 v[8:11], v[172:175], v[56:59], v[8:11]
  v_fma_f32 v105, v105, v45, v37
  v_fma_f32 v106, v106, v46, v38
  ds_read_b128 v[96:99], v2 offset:80
  ds_read_b128 v[100:103], v3 offset:80
  v_mfma_f32_16x16x32_bf16 v[12:15], v[176:179], v[56:59], v[12:15]
  v_fma_f32 v107, v107, v47, v39
  v_fma_f32 v108, v108, v48, v40
  v_mfma_f32_16x16x32_bf16 v[16:19], v[56:59], v[180:183], v[16:19]
  v_fma_f32 v109, v109, v49, v41
  v_fma_f32 v110, v110, v50, v42
  v_fma_f32 v111, v111, v51, v43
  v_mfma_f32_16x16x32_bf16 v[20:23], v[176:179], v[28:31], v[20:23]
  v_cvt_pk_bf16_f32 v104, v104, v105
  v_cvt_pk_bf16_f32 v106, v106, v107
  v_mfma_f32_16x16x32_bf16 v[24:27], v[28:31], v[180:183], v[24:27]
  v_cvt_pk_bf16_f32 v108, v108, v109
  v_cvt_pk_bf16_f32 v110, v110, v111
  v_mfma_f32_16x16x32_bf16 v[8:11], v[184:187], v[60:63], v[8:11]
  ds_write_addtid_b32 v104 offset:16640
  ds_write_addtid_b32 v106 offset:16896
  ds_read_b128 v[56:59], v2 offset:96
  ds_read_b128 v[28:31], v3 offset:96
  v_mfma_f32_16x16x32_bf16 v[12:15], v[188:191], v[60:63], v[12:15]
  ds_write_addtid_b32 v108 offset:17152
  ds_write_addtid_b32 v110 offset:17408
  v_fma_f32 v112, v112, v44, v36
  v_mfma_f32_16x16x32_bf16 v[16:19], v[60:63], v[192:195], v[16:19]
  v_fma_f32 v113, v113, v45, v37
  v_fma_f32 v114, v114, v46, v38
  v_mfma_f32_16x16x32_bf16 v[20:23], v[188:191], v[32:35], v[20:23]
  v_fma_f32 v115, v115, v47, v39
  v_fma_f32 v116, v116, v48, v40
  v_mfma_f32_16x16x32_bf16 v[24:27], v[32:35], v[192:195], v[24:27]
  v_fma_f32 v117, v117, v49, v41
  v_fma_f32 v118, v118, v50, v42
  s_waitcnt lgkmcnt(7)
  v_mfma_f32_16x16x32_bf16 v[8:11], v[196:199], v[96:99], v[8:11]
  v_fma_f32 v119, v119, v51, v43
  v_cvt_pk_bf16_f32 v112, v112, v113
  v_cvt_pk_bf16_f32 v114, v114, v115
  ds_read_b128 v[60:63], v2 offset:112
  ds_read_b128 v[32:35], v3 offset:112
  v_mfma_f32_16x16x32_bf16 v[12:15], v[200:203], v[96:99], v[12:15]
  v_cvt_pk_bf16_f32 v116, v116, v117
  v_cvt_pk_bf16_f32 v118, v118, v119
  v_mfma_f32_16x16x32_bf16 v[16:19], v[96:99], v[204:207], v[16:19]
  ds_write_addtid_b32 v112 offset:17680
  ds_write_addtid_b32 v114 offset:17936
  s_waitcnt lgkmcnt(10)
  v_mfma_f32_16x16x32_bf16 v[20:23], v[200:203], v[100:103], v[20:23]
  ds_write_addtid_b32 v116 offset:18192
  ds_write_addtid_b32 v118 offset:18448
  v_mfma_f32_16x16x32_bf16 v[24:27], v[100:103], v[204:207], v[24:27]
  v_fma_f32 v120, v120, v44, v36
  v_fma_f32 v121, v121, v45, v37
  v_fma_f32 v122, v122, v46, v38
  s_waitcnt lgkmcnt(9)
  v_mfma_f32_16x16x32_bf16 v[8:11], v[208:211], v[56:59], v[8:11]
  v_fma_f32 v123, v123, v47, v39
  v_fma_f32 v124, v124, v48, v40
  ds_read_b128 v[96:99], v2 offset:128
  ds_read_b128 v[100:103], v3 offset:128
  v_mfma_f32_16x16x32_bf16 v[12:15], v[212:215], v[56:59], v[12:15]
  v_fma_f32 v125, v125, v49, v41
  v_fma_f32 v126, v126, v50, v42
  v_mfma_f32_16x16x32_bf16 v[16:19], v[56:59], v[216:219], v[16:19]
  v_fma_f32 v127, v127, v51, v43
  v_cvt_pk_bf16_f32 v120, v120, v121
  v_cvt_pk_bf16_f32 v122, v122, v123
  s_waitcnt lgkmcnt(10)
  v_mfma_f32_16x16x32_bf16 v[20:23], v[212:215], v[28:31], v[20:23]
  v_cvt_pk_bf16_f32 v124, v124, v125
  v_cvt_pk_bf16_f32 v126, v126, v127
  v_mfma_f32_16x16x32_bf16 v[24:27], v[28:31], v[216:219], v[24:27]
  ds_write_addtid_b32 v120 offset:33280
  ds_write_addtid_b32 v122 offset:33536
  s_waitcnt lgkmcnt(9)
  v_mfma_f32_16x16x32_bf16 v[8:11], v[220:223], v[60:63], v[8:11]
  ds_write_addtid_b32 v124 offset:33792
  ds_write_addtid_b32 v126 offset:34048
  ds_read_b128 v[56:59], v2 offset:144
  ds_read_b128 v[28:31], v3 offset:144
  v_mfma_f32_16x16x32_bf16 v[12:15], v[224:227], v[60:63], v[12:15]
  v_fma_f32 v128, v128, v44, v36
  v_fma_f32 v129, v129, v45, v37
  v_fma_f32 v130, v130, v46, v38
  v_mfma_f32_16x16x32_bf16 v[16:19], v[60:63], v[228:231], v[16:19]
  v_fma_f32 v131, v131, v47, v39
  v_fma_f32 v132, v132, v48, v40
  s_waitcnt lgkmcnt(12)
  v_mfma_f32_16x16x32_bf16 v[20:23], v[224:227], v[32:35], v[20:23]
  v_fma_f32 v133, v133, v49, v41
  v_fma_f32 v134, v134, v50, v42
  v_mfma_f32_16x16x32_bf16 v[24:27], v[32:35], v[228:231], v[24:27]
  v_fma_f32 v135, v135, v51, v43
  v_cvt_pk_bf16_f32 v128, v128, v129
  s_waitcnt lgkmcnt(7)
  v_mfma_f32_16x16x32_bf16 v[8:11], v[232:235], v[96:99], v[8:11]
  v_cvt_pk_bf16_f32 v130, v130, v131
  v_cvt_pk_bf16_f32 v132, v132, v133
  v_cvt_pk_bf16_f32 v134, v134, v135
  ds_read_b128 v[60:63], v2 offset:160
  ds_read_b128 v[32:35], v3 offset:160
  v_mfma_f32_16x16x32_bf16 v[12:15], v[236:239], v[96:99], v[12:15]
  ds_write_addtid_b32 v128 offset:34320
  ds_write_addtid_b32 v130 offset:34576
  v_mfma_f32_16x16x32_bf16 v[16:19], v[96:99], v[240:243], v[16:19]
  ds_write_addtid_b32 v132 offset:34832
  ds_write_addtid_b32 v134 offset:35088
  s_waitcnt lgkmcnt(12)
  v_mfma_f32_16x16x32_bf16 v[20:23], v[236:239], v[100:103], v[20:23]
  v_mov_b32 v4, v7
  s_bitcmp1_b32 21, s48
  s_cselect_b32 s55, 0, 0x40000000
  s_mov_b32 exec_lo, 0
  v_add_u32 v4, s55, v7
  s_mov_b32 exec_lo, -1
  s_mov_b32 s72, 0x3eb8aa3b
  v_mfma_f32_16x16x32_bf16 v[24:27], v[100:103], v[240:243], v[24:27]
  s_waitcnt vmcnt(2)
  s_waitcnt lgkmcnt(0)
  s_barrier
  ds_read_b64_tr_b16 v[88:89], v53 offset:21760
  ds_read_b64_tr_b16 v[90:91], v53 offset:27904
  v_mfma_f32_16x16x32_bf16 v[8:11], v[244:247], v[56:59], v[8:11]
  ds_read_b64_tr_b16 v[92:93], v53 offset:22784
  ds_read_b64_tr_b16 v[94:95], v53 offset:28928
  ds_read_b128 v[40:43], v2 offset:176
  ds_read_b128 v[44:47], v3 offset:176
  v_mfma_f32_16x16x32_bf16 v[12:15], v[248:251], v[56:59], v[12:15]
  ds_read_b64_tr_b16 v[96:97], v53 offset:23808
  ds_read_b64_tr_b16 v[98:99], v53 offset:29952
  v_mfma_f32_16x16x32_bf16 v[16:19], v[56:59], v[252:255], v[16:19]
  ds_read_b64_tr_b16 v[100:101], v53 offset:34048
  ds_read_b64_tr_b16 v[102:103], v53 offset:40192
  v_mfma_f32_16x16x32_bf16 v[20:23], v[248:251], v[28:31], v[20:23]
  ds_read_b64_tr_b16 v[104:105], v53 offset:35072
  ds_read_b64_tr_b16 v[106:107], v53 offset:41216
  v_mfma_f32_16x16x32_bf16 v[24:27], v[28:31], v[252:255], v[24:27]
  ds_read_b64_tr_b16 v[108:109], v53 offset:36096
  ds_read_b64_tr_b16 v[110:111], v53 offset:42240
  v_mfma_f32_16x16x32_bf16 v[8:11], v[64:67], v[60:63], v[8:11]
  ds_read_b64_tr_b16 v[112:113], v53 offset:46336
  ds_read_b64_tr_b16 v[114:115], v53 offset:52480
  ds_read_b128 v[56:59], v2 offset:192
  ds_read_b128 v[28:31], v3 offset:192
  v_mfma_f32_16x16x32_bf16 v[12:15], v[68:71], v[60:63], v[12:15]
  ds_read_b64_tr_b16 v[116:117], v53 offset:47360
  ds_read_b64_tr_b16 v[118:119], v53 offset:53504
  v_mfma_f32_16x16x32_bf16 v[16:19], v[60:63], v[72:75], v[16:19]
  ds_read_b64_tr_b16 v[120:121], v53 offset:48384
  ds_read_b64_tr_b16 v[122:123], v53 offset:54528
  v_mfma_f32_16x16x32_bf16 v[20:23], v[68:71], v[32:35], v[20:23]
  ds_read_b64_tr_b16 v[124:125], v53 offset:58624
  ds_read_b64_tr_b16 v[126:127], v54 offset:3328
  v_mfma_f32_16x16x32_bf16 v[24:27], v[32:35], v[72:75], v[24:27]
  ds_read_b64_tr_b16 v[128:129], v53 offset:59648
  ds_read_b64_tr_b16 v[130:131], v54 offset:4352
  s_waitcnt lgkmcnt(15)
  v_mfma_f32_16x16x32_bf16 v[8:11], v[76:79], v[40:43], v[8:11]
  ds_read_b64_tr_b16 v[132:133], v53 offset:60672
  ds_read_b64_tr_b16 v[134:135], v54 offset:5376
  ds_read_b128 v[60:63], v2 offset:208
  ds_read_b128 v[32:35], v3 offset:208
  v_mfma_f32_16x16x32_bf16 v[12:15], v[80:83], v[40:43], v[12:15]
  v_mfma_f32_16x16x32_bf16 v[16:19], v[40:43], v[84:87], v[16:19]
  s_waitcnt lgkmcnt(15)
  v_mfma_f32_16x16x32_bf16 v[20:23], v[80:83], v[44:47], v[20:23]
  v_mfma_f32_16x16x32_bf16 v[24:27], v[44:47], v[84:87], v[24:27]
  s_waitcnt lgkmcnt(0)
  s_barrier
  s_add_u32 m0, s96, 0x14500
  s_xor_b32 s59, s95, 0
  buffer_load_dwordx4 v5, s[12:15], s59 offen lds
  v_mfma_f32_16x16x32_bf16 v[8:11], v[88:91], v[56:59], v[8:11]
  ds_read_b128 v[40:43], v2 offset:224
  ds_read_b128 v[44:47], v3 offset:224
  v_mfma_f32_16x16x32_bf16 v[12:15], v[92:95], v[56:59], v[12:15]
  v_mfma_f32_16x16x32_bf16 v[16:19], v[56:59], v[96:99], v[16:19]
  s_add_u32 m0, s96, 0x14900
  s_xor_b32 s59, s95, 0x4000
  buffer_load_dwordx4 v5, s[12:15], s59 offen lds
  v_mfma_f32_16x16x32_bf16 v[20:23], v[92:95], v[28:31], v[20:23]
  v_mfma_f32_16x16x32_bf16 v[24:27], v[28:31], v[96:99], v[24:27]
  v_mfma_f32_16x16x32_bf16 v[8:11], v[100:103], v[60:63], v[8:11]
  s_add_u32 m0, s96, 0x18500
  s_xor_b32 s59, s95, 0x8000
  buffer_load_dwordx4 v5, s[12:15], s59 offen lds
  ds_read_b128 v[56:59], v2 offset:240
  ds_read_b128 v[28:31], v3 offset:240
  v_mfma_f32_16x16x32_bf16 v[12:15], v[104:107], v[60:63], v[12:15]
  v_mfma_f32_16x16x32_bf16 v[16:19], v[60:63], v[108:111], v[16:19]
  v_mfma_f32_16x16x32_bf16 v[20:23], v[104:107], v[32:35], v[20:23]
  s_add_u32 m0, s96, 0x18900
  s_xor_b32 s59, s95, 0xc000
  buffer_load_dwordx4 v5, s[12:15], s59 offen lds
  v_mfma_f32_16x16x32_bf16 v[24:27], v[32:35], v[108:111], v[24:27]
  s_waitcnt lgkmcnt(3)
  v_mfma_f32_16x16x32_bf16 v[8:11], v[112:115], v[40:43], v[8:11]
  ds_read_b128 v[60:63], v2 offset:33440
  v_mfma_f32_16x16x32_bf16 v[12:15], v[116:119], v[40:43], v[12:15]
  s_add_u32 m0, s96, 0x1c500
  s_xor_b32 s59, s95, 0x10000
  buffer_load_dwordx4 v5, s[12:15], s59 offen lds
  v_mfma_f32_16x16x32_bf16 v[16:19], v[40:43], v[120:123], v[16:19]
  s_waitcnt lgkmcnt(3)
  v_mfma_f32_16x16x32_bf16 v[20:23], v[116:119], v[44:47], v[20:23]
  v_mfma_f32_16x16x32_bf16 v[24:27], v[44:47], v[120:123], v[24:27]
  s_add_u32 m0, s96, 0x1c900
  s_xor_b32 s59, s95, 0x14000
  buffer_load_dwordx4 v5, s[12:15], s59 offen lds
  s_waitcnt lgkmcnt(2)
  v_mfma_f32_16x16x32_bf16 v[8:11], v[124:127], v[56:59], v[8:11]
  v_mfma_f32_16x16x32_bf16 v[12:15], v[128:131], v[56:59], v[12:15]
  v_mfma_f32_16x16x32_bf16 v[16:19], v[56:59], v[132:135], v[16:19]
  ds_read_b128 v[56:59], v2 offset:33456
  s_waitcnt lgkmcnt(2)
  v_mfma_f32_16x16x32_bf16 v[20:23], v[128:131], v[28:31], v[20:23]
  v_mfma_f32_16x16x32_bf16 v[24:27], v[28:31], v[132:135], v[24:27]
  s_waitcnt lgkmcnt(0)
  s_barrier
  s_xor_b32 s59, s31, 0
  s_mul_i32 s59, s59, 0x56000
  s_add_u32 s59, s59, s99
  s_add_u32 m0, s51, 0
  s_nop 0
  buffer_load_dwordx4 v7, s[16:19], s59 offen lds
  v_mfma_f32_16x16x32_bf16 v[36:39], v[64:67], v[60:63], 0
  s_andn2_b32 s55, 0xcf6e6b28, s30
  v_mov_b32 v54, s55
  v_cvt_pk_bf16_f32 v28, v8, v9
  v_cvt_pk_bf16_f32 v29, v10, v11
  ds_read_b128 v[64:67], v2 offset:33280
  v_mfma_f32_16x16x32_bf16 v[40:43], v[68:71], v[60:63], 0
  s_xor_b32 s59, s31, 0
  s_mul_i32 s59, s59, 0x56000
  s_add_u32 s59, s59, s99
  s_add_u32 m0, s51, 0x380
  s_nop 0
  buffer_load_dwordx4 v7, s[16:19], s59 offen offset:128 lds
  v_cvt_pk_bf16_f32 v32, v12, v13
  v_cvt_pk_bf16_f32 v33, v14, v15
  v_cvt_pk_bf16_f32 v30, v20, v21
  ds_read_b128 v[68:71], v2 offset:33296
  v_mfma_f32_16x16x32_bf16 v[44:47], v[60:63], v[72:75], 0
  v_cvt_pk_bf16_f32 v31, v22, v23
  s_mov_b32 vcc_lo, 0xff000ff
  s_mov_b32 vcc_hi, 0xf000ff00
  v_cndmask_b32_e64 v12, v55, 0, vcc
  s_mov_b32 vcc_lo, 0x1fe001fe
  s_mov_b32 vcc_hi, 0xe000fe00
  v_cndmask_b32_e64 v13, v55, 0, vcc
  ds_read_b128 v[72:75], v2 offset:33312
  v_mfma_f32_16x16x32_bf16 v[36:39], v[76:79], v[56:59], v[36:39]
  s_mov_b32 vcc_lo, 0x3fc003fc
  s_mov_b32 vcc_hi, 0xc000fc00
  v_cndmask_b32_e64 v14, v55, 0, vcc
  s_mov_b32 vcc_lo, 0x7f8007f8
  s_mov_b32 vcc_hi, 0x8000f800
  v_cndmask_b32_e64 v15, v55, 0, vcc
  ds_read_b128 v[76:79], v2 offset:33328
  v_mfma_f32_16x16x32_bf16 v[40:43], v[80:83], v[56:59], v[40:43]
  s_xor_b32 s59, s31, 0
  s_mul_i32 s59, s59, 0x56000
  s_add_u32 s59, s59, s99
  s_add_u32 m0, s51, 0x700
  s_nop 0
  buffer_load_dwordx4 v4, s[16:19], s59 offen offset:256 lds
  s_mov_b32 vcc_lo, 0
  s_mov_b32 vcc_hi, 0xf0000
  v_cndmask_b32_e64 v8, v55, v54, vcc
  s_mov_b32 vcc_lo, 0
  s_mov_b32 vcc_hi, 0x1f0001
  v_cndmask_b32_e64 v9, v55, v54, vcc
  s_mov_b32 vcc_lo, 0
  s_mov_b32 vcc_hi, 0x3f0003
  v_cndmask_b32_e64 v10, v55, v54, vcc
  ds_read_b128 v[80:83], v2 offset:33344
  v_mfma_f32_16x16x32_bf16 v[44:47], v[56:59], v[84:87], v[44:47]
  s_mov_b32 vcc_lo, 0
  s_mov_b32 vcc_hi, 0x7f0007
  v_cndmask_b32_e64 v11, v55, v54, vcc
  v_and_b32 v30, s30, v30
  v_and_b32 v31, s30, v31
  ds_read_b128 v[84:87], v2 offset:33360
  s_waitcnt lgkmcnt(5)
  v_mfma_f32_16x16x32_bf16 v[36:39], v[136:139], v[64:67], v[36:39]
  s_xor_b32 s59, s31, 0
  s_mul_i32 s59, s59, 0x56000
  s_add_u32 s59, s59, s99
  s_add_u32 s59, s59, 0x2b000
  s_add_u32 m0, s51, 0x6000
  s_nop 0
  buffer_load_dwordx4 v7, s[16:19], s59 offen lds
  v_mfma_f32_16x16x16_bf16 v[12:15], v[32:33], v[28:29], v[12:15]
  v_mfma_f32_16x16x16_bf16 v[8:11], v[30:31], v[28:29], v[8:11]
  v_cvt_pk_bf16_f32 v48, v24, v25
  ds_read_b128 v[60:63], v2 offset:33408
  v_mfma_f32_16x16x32_bf16 v[40:43], v[140:143], v[64:67], v[40:43]
  v_cvt_pk_bf16_f32 v49, v26, v27
  v_cvt_pk_bf16_f32 v50, v16, v17
  v_mfma_f32_16x16x32_bf16 v[44:47], v[64:67], v[144:147], v[44:47]
  v_cvt_pk_bf16_f32 v51, v18, v19
  v_mov_b32 v20, 0x3f803f80
  v_mov_b32 v21, 0x3f803f80
  s_waitcnt lgkmcnt(5)
  v_mfma_f32_16x16x32_bf16 v[36:39], v[148:151], v[68:71], v[36:39]
  s_xor_b32 s59, s31, 0
  s_mul_i32 s59, s59, 0x56000
  s_add_u32 s59, s59, s99
  s_add_u32 s59, s59, 0x2b000
  s_add_u32 m0, s51, 0x6380
  s_nop 0
  buffer_load_dwordx4 v7, s[16:19], s59 offen offset:128 lds
  v_mov_b32 v22, 0x3f803f80
  v_mov_b32 v23, 0x3f803f80
  v_and_b32 v3, 0xf0, v1
  ds_read_b128 v[56:59], v2 offset:33424
  v_mfma_f32_16x16x32_bf16 v[40:43], v[152:155], v[68:71], v[40:43]
  v_lshrrev_b32 v34, 5, v0
  v_lshl_add_u32 v3, v34, 8, v3
  v_bfe_u32 v34, v0, 4, 1
  v_mfma_f32_16x16x32_bf16 v[44:47], v[68:71], v[156:159], v[44:47]
  s_xor_b32 s59, s31, 0
  s_mul_i32 s59, s59, 0x56000
  s_add_u32 s59, s59, s99
  s_add_u32 s59, s59, 0x2b000
  s_add_u32 m0, s51, 0x6700
  s_nop 0
  buffer_load_dwordx4 v4, s[16:19], s59 offen offset:256 lds
  v_lshl_add_u32 v3, v34, 3, v3
  v_max3_f32 v34, v12, v13, v14
  s_waitcnt lgkmcnt(5)
  v_mfma_f32_16x16x32_bf16 v[36:39], v[160:163], v[72:75], v[36:39]
  v_max3_f32 v34, v34, v15, v8
  v_max3_f32 v34, v34, v9, v10
  v_max_f32 v34, v34, v11
  ds_read_b128 v[64:67], v2 offset:33376
  v_mfma_f32_16x16x32_bf16 v[40:43], v[164:167], v[72:75], v[40:43]
  v_mov_b32 v35, v34
  s_nop 1
  v_permlane32_swap_b32 v34, v35
  v_max_f32 v34, v34, v35
  v_mfma_f32_16x16x32_bf16 v[44:47], v[72:75], v[168:171], v[44:47]
  v_mov_b32 v35, v34
  s_nop 1
  v_permlane16_swap_b32 v34, v35
  s_waitcnt lgkmcnt(5)
  v_mfma_f32_16x16x32_bf16 v[36:39], v[172:175], v[76:79], v[36:39]
  v_max_f32 v34, v34, v35
  v_mul_f32 v34, 0xbeb8aa3b, v34
  v_fma_f32 v8, v8, s72, v34
  ds_read_b128 v[68:71], v2 offset:33392
  v_mfma_f32_16x16x32_bf16 v[40:43], v[176:179], v[76:79], v[40:43]
  v_fma_f32 v9, v9, s72, v34
  v_fma_f32 v10, v10, s72, v34
  v_fma_f32 v11, v11, s72, v34
  v_mfma_f32_16x16x32_bf16 v[44:47], v[76:79], v[180:183], v[44:47]
  v_fma_f32 v12, v12, s72, v34
  v_fma_f32 v13, v13, s72, v34
  v_fma_f32 v14, v14, s72, v34
  s_waitcnt lgkmcnt(5)
  v_mfma_f32_16x16x32_bf16 v[36:39], v[184:187], v[80:83], v[36:39]
  v_fma_f32 v15, v15, s72, v34
  v_exp_f32 v8, v8
  ds_read_b128 v[72:75], v2 offset:33472
  v_mfma_f32_16x16x32_bf16 v[40:43], v[188:191], v[80:83], v[40:43]
  v_exp_f32 v9, v9
  v_exp_f32 v10, v10
  v_exp_f32 v11, v11
  v_mfma_f32_16x16x32_bf16 v[44:47], v[80:83], v[192:195], v[44:47]
  v_exp_f32 v12, v12
  v_exp_f32 v13, v13
  v_exp_f32 v14, v14
  s_waitcnt lgkmcnt(5)
  v_mfma_f32_16x16x32_bf16 v[36:39], v[196:199], v[84:87], v[36:39]
  v_exp_f32 v15, v15
  v_cvt_pk_bf16_f32 v16, v8, v9
  v_cvt_pk_bf16_f32 v17, v10, v11
  ds_read_b128 v[76:79], v2 offset:33488
  v_mfma_f32_16x16x32_bf16 v[40:43], v[200:203], v[84:87], v[40:43]
  v_cvt_pk_bf16_f32 v18, v12, v13
  v_cvt_pk_bf16_f32 v19, v14, v15
  v_mfma_f32_16x16x32_bf16 v[44:47], v[84:87], v[204:207], v[44:47]
  s_nop 0
  v_mfma_f32_16x16x32_bf16 v[8:11], v[20:23], v[16:19], 0
  v_mfma_f32_16x16x32_bf16 v[24:27], v[48:51], v[16:19], 0
  s_nop 6
  v_rcp_f32 v8, v8
  v_lshrrev_b32 v202, 5, v0
  s_lshl_b32 s55, s46, 12
  s_add_u32 s55, s55, 0x14500
  v_lshl_add_u32 v202, v202, 10, s55
  s_waitcnt lgkmcnt(5)
  v_mfma_f32_16x16x32_bf16 v[36:39], v[232:235], v[60:63], v[36:39]
  v_mul_f32 v24, v24, v8
  v_mul_f32 v25, v25, v8
  v_mul_f32 v26, v26, v8
  v_bfe_u32 v203, v0, 2, 3
  v_lshl_add_u32 v202, v203, 6, v202
  ds_read_b128 v[80:83], v2 offset:33504
  v_mfma_f32_16x16x32_bf16 v[40:43], v[236:239], v[60:63], v[40:43]
  v_mul_f32 v27, v27, v8
  v_cvt_pk_bf16_f32 v24, v24, v25
  v_cvt_pk_bf16_f32 v25, v26, v27
  v_bfe_u32 v203, v0, 4, 1
  v_lshl_add_u32 v202, v203, 5, v202
  v_mfma_f32_16x16x32_bf16 v[44:47], v[60:63], v[240:243], v[44:47]
  v_min_u32 v24, -2, v24
  s_lshl_b32 s55, s31, 10
  s_xor_b32 s55, s55, s97
  s_add_u32 s55, s55, 0x8000
  buffer_store_dwordx2 v[24:25], v3, s[40:43], s55 offen sc1
  v_and_b32 v203, 3, v0
  v_lshl_add_u32 v202, v203, 3, v202
  s_waitcnt lgkmcnt(5)
  v_mfma_f32_16x16x32_bf16 v[36:39], v[244:247], v[56:59], v[36:39]
  s_mov_b32 vcc_lo, 0xff000ff
  s_mov_b32 vcc_hi, 0xf000ff00
  v_cndmask_b32_e64 v12, v55, 0, vcc
  s_mov_b32 vcc_lo, 0x1fe001fe
  s_mov_b32 vcc_hi, 0xe000fe00
  v_cndmask_b32_e64 v13, v55, 0, vcc
  s_mov_b32 vcc_lo, 0x3fc003fc
  s_mov_b32 vcc_hi, 0xc000fc00
  v_cndmask_b32_e64 v14, v55, 0, vcc
  v_xor_b32 v203, 32, v202
  s_waitcnt vmcnt(7)
  s_barrier
  ds_read_b128 v[84:87], v2 offset:33520
  v_mfma_f32_16x16x32_bf16 v[40:43], v[248:251], v[56:59], v[40:43]
  s_mov_b32 vcc_lo, 0x7f8007f8
  s_mov_b32 vcc_hi, 0x8000f800
  v_cndmask_b32_e64 v15, v55, 0, vcc
  s_mov_b32 vcc_lo, 0
  s_mov_b32 vcc_hi, 0xf0000
  v_cndmask_b32_e64 v8, v55, 0, vcc
  s_mov_b32 vcc_lo, 0
  s_mov_b32 vcc_hi, 0x1f0001
  v_cndmask_b32_e64 v9, v55, 0, vcc
  ds_read_b64_tr_b16 v[196:197], v203 offset:49664
  ds_read_b64_tr_b16 v[198:199], v203 offset:51712
  v_mfma_f32_16x16x32_bf16 v[44:47], v[56:59], v[252:255], v[44:47]
  s_mov_b32 vcc_lo, 0
  s_mov_b32 vcc_hi, 0x3f0003
  v_cndmask_b32_e64 v10, v55, 0, vcc
  s_mov_b32 vcc_lo, 0
  s_mov_b32 vcc_hi, 0x7f0007
  v_cndmask_b32_e64 v11, v55, 0, vcc
  ds_read_b64_tr_b16 v[192:193], v202 offset:49664
  ds_read_b64_tr_b16 v[194:195], v202 offset:51712
  v_and_b32 v201, 1, v0
  v_lshlrev_b32 v201, 6, v201
  s_waitcnt lgkmcnt(9)
  v_mfma_f32_16x16x32_bf16 v[36:39], v[208:211], v[64:67], v[36:39]
  ds_read_b64_tr_b16 v[188:189], v203 offset:49152
  ds_read_b64_tr_b16 v[190:191], v203 offset:51200
  s_lshl_b32 s74, s46, 7
  buffer_load_dword v200, v201, s[40:43], s74 offen sc1
  v_mfma_f32_16x16x32_bf16 v[40:43], v[212:215], v[64:67], v[40:43]
  ds_read_b64_tr_b16 v[184:185], v202 offset:49152
  ds_read_b64_tr_b16 v[186:187], v202 offset:51200
  v_and_b32 v248, 15, v0
  v_lshlrev_b32 v248, 11, v248
  v_mfma_f32_16x16x32_bf16 v[44:47], v[64:67], v[216:219], v[44:47]
  ds_read_b64_tr_b16 v[148:149], v203 offset:512
  ds_read_b64_tr_b16 v[150:151], v203 offset:2560
  v_lshrrev_b32 v251, 4, v0
  s_waitcnt lgkmcnt(14)
  v_mfma_f32_16x16x32_bf16 v[36:39], v[220:223], v[68:71], v[36:39]
  ds_read_b64_tr_b16 v[144:145], v202 offset:512
  ds_read_b64_tr_b16 v[146:147], v202 offset:2560
  v_lshl_add_u32 v248, v251, 3, v248
  v_mfma_f32_16x16x32_bf16 v[40:43], v[224:227], v[68:71], v[40:43]
  ds_read_b64_tr_b16 v[140:141], v203
  buffer_load_dwordx2 v[234:235], v248, s[4:7], s98 offen
  s_add_u32 s55, s98, 0x8000
  buffer_load_dwordx2 v[238:239], v248, s[4:7], s55 offen
  v_mfma_f32_16x16x32_bf16 v[44:47], v[68:71], v[228:231], v[44:47]
  ds_read_b64_tr_b16 v[142:143], v203 offset:2048
  ds_read_b64_tr_b16 v[136:137], v202
  s_lshl_b32 s55, s46, 5
  s_add_u32 s55, s55, 0x27d00
  v_lshl_add_u32 v251, v251, 3, s55
  s_waitcnt lgkmcnt(15)
  v_mfma_f32_16x16x32_bf16 v[36:39], v[88:91], v[72:75], v[36:39]
  ds_read_b64_tr_b16 v[138:139], v202 offset:2048
  ds_read_b64_tr_b16 v[164:165], v203 offset:16896
  ds_read_b64 v[242:243], v251
  s_lshl_b32 s55, s31, 1
  s_xor_b32 s55, s55, s48
  s_lshl_b32 s55, s55, 7
  s_sub_u32 s55, s55, 0x400
  v_add_u32 v251, s55, v251
  v_mfma_f32_16x16x32_bf16 v[40:43], v[92:95], v[72:75], v[40:43]
  ds_read_b64_tr_b16 v[166:167], v203 offset:18944
  ds_read_b64_tr_b16 v[160:161], v202 offset:16896
  ds_read_b64 v[246:247], v251
  v_mfma_f32_16x16x32_bf16 v[44:47], v[72:75], v[96:99], v[44:47]
  ds_read_b64_tr_b16 v[162:163], v202 offset:18944
  ds_read_b64_tr_b16 v[156:157], v203 offset:16384
  s_xor_b32 s56, s44, 1
  s_mul_i32 s56, s56, 0x1800
  s_add_u32 s56, s56, 0x1a500
  v_add_u32 v252, s56, v1
  s_waitcnt lgkmcnt(15)
  v_mfma_f32_16x16x32_bf16 v[36:39], v[100:103], v[76:79], v[36:39]
  ds_read_b64_tr_b16 v[158:159], v203 offset:18432
  ds_read_b64_tr_b16 v[152:153], v202 offset:16384
  s_xor_b32 s56, s44, 2
  s_mul_i32 s56, s56, 0x1800
  s_add_u32 s56, s56, 0x1a900
  v_add_u32 v253, s56, v1
  s_xor_b32 s56, s44, 3
  s_mul_i32 s56, s56, 0x1800
  s_add_u32 s56, s56, 0x1ad00
  v_add_u32 v254, s56, v1
  v_mfma_f32_16x16x32_bf16 v[40:43], v[104:107], v[76:79], v[40:43]
  ds_read_b64_tr_b16 v[154:155], v202 offset:18432
  ds_read_b64_tr_b16 v[180:181], v203 offset:33280
  s_mul_i32 s56, s44, 0x1800
  s_add_u32 s56, s56, 0x1a500
  v_add_u32 v255, s56, v1
  v_mfma_f32_16x16x32_bf16 v[44:47], v[76:79], v[108:111], v[44:47]
  ds_read_b64_tr_b16 v[182:183], v203 offset:35328
  ds_read_b64_tr_b16 v[176:177], v202 offset:33280
  s_waitcnt lgkmcnt(11)
  v_lshlrev_b32 v240, 16, v242
  v_and_b32 v241, 0xffff0000, v242
  v_mfma_f32_16x16x32_bf16 v[36:39], v[112:115], v[80:83], v[36:39]
  ds_read_b64_tr_b16 v[178:179], v202 offset:35328
  ds_read_b64_tr_b16 v[172:173], v203 offset:32768
  v_lshlrev_b32 v242, 16, v243
  v_mfma_f32_16x16x32_bf16 v[40:43], v[116:119], v[80:83], v[40:43]
  ds_read_b64_tr_b16 v[174:175], v203 offset:34816
  ds_read_b64_tr_b16 v[168:169], v202 offset:32768
  v_and_b32 v243, 0xffff0000, v243
  v_mfma_f32_16x16x32_bf16 v[44:47], v[80:83], v[120:123], v[44:47]
  ds_read_b64_tr_b16 v[170:171], v202 offset:34816
  v_mov_b32 v249, v202
  s_waitcnt lgkmcnt(13)
  v_lshlrev_b32 v244, 16, v246
  v_and_b32 v245, 0xffff0000, v246
  v_mfma_f32_16x16x32_bf16 v[36:39], v[124:127], v[84:87], v[36:39]
  v_mov_b32 v250, v203
  v_lshlrev_b32 v246, 16, v247
  v_mfma_f32_16x16x32_bf16 v[40:43], v[128:131], v[84:87], v[40:43]
  v_and_b32 v247, 0xffff0000, v247
  v_mfma_f32_16x16x32_bf16 v[44:47], v[84:87], v[132:135], v[44:47]
  s_nop 2
  v_cvt_pk_bf16_f32 v28, v36, v37
  v_cvt_pk_bf16_f32 v29, v38, v39
  s_nop 0
  v_cvt_pk_bf16_f32 v30, v40, v41
  v_cvt_pk_bf16_f32 v31, v42, v43
  v_mfma_f32_16x16x16_bf16 v[8:11], v[32:33], v[28:29], v[8:11]
  s_nop 0
  v_mfma_f32_16x16x16_bf16 v[12:15], v[30:31], v[28:29], v[12:15]
  v_cvt_pk_bf16_f32 v52, v44, v45
  v_cvt_pk_bf16_f32 v53, v46, v47
  s_nop 3
  v_max3_f32 v34, v8, v9, v10
  s_nop 0
  v_max3_f32 v34, v34, v11, v12
  v_max3_f32 v34, v34, v13, v14
  v_max_f32 v34, v34, v15
  v_mov_b32 v35, v34
  s_nop 1
  v_permlane32_swap_b32 v34, v35
  v_max_f32 v34, v34, v35
  v_mov_b32 v35, v34
  s_nop 1
  v_permlane16_swap_b32 v34, v35
  v_max_f32 v34, v34, v35
  v_mul_f32 v34, 0xbeb8aa3b, v34
  v_fma_f32 v8, v8, s72, v34
  v_fma_f32 v9, v9, s72, v34
  v_fma_f32 v10, v10, s72, v34
  v_fma_f32 v11, v11, s72, v34
  v_fma_f32 v12, v12, s72, v34
  v_fma_f32 v13, v13, s72, v34
  v_fma_f32 v14, v14, s72, v34
  v_fma_f32 v15, v15, s72, v34
  v_exp_f32 v8, v8
  v_exp_f32 v9, v9
  v_exp_f32 v10, v10
  v_exp_f32 v11, v11
  v_exp_f32 v12, v12
  v_exp_f32 v13, v13
  v_exp_f32 v14, v14
  v_exp_f32 v15, v15
  v_cvt_pk_bf16_f32 v16, v8, v9
  v_cvt_pk_bf16_f32 v17, v10, v11
  v_cvt_pk_bf16_f32 v18, v12, v13
  v_cvt_pk_bf16_f32 v19, v14, v15
  s_nop 1
  v_mfma_f32_16x16x32_bf16 v[8:11], v[20:23], v[16:19], 0
  v_mfma_f32_16x16x32_bf16 v[24:27], v[50:53], v[16:19], 0
  s_nop 6
  v_rcp_f32 v8, v8
  s_nop 0
  v_mul_f32 v24, v24, v8
  v_mul_f32 v25, v25, v8
  v_mul_f32 v26, v26, v8
  v_mul_f32 v27, v27, v8
  v_cvt_pk_bf16_f32 v24, v24, v25
  v_cvt_pk_bf16_f32 v25, v26, v27
  v_min_u32 v24, -2, v24
  s_lshl_b32 s55, s31, 10
  s_xor_b32 s55, s55, s97
  s_add_u32 s55, s55, 0xc000
  buffer_store_dwordx2 v[24:25], v3, s[40:43], s55 offen sc1
Lck7_%=:
  s_waitcnt vmcnt(3)
  v_cmp_ne_u32 vcc, s49, v200
  s_and_b64 vcc, vcc, s[0:1]
  s_cbranch_scc1 Lsl8_%=
  buffer_load_dwordx4 v[200:203], v1, s[40:43], s28 offen sc1
  buffer_load_dwordx4 v[204:207], v1, s[40:43], s28 offen offset:1024 sc1
  buffer_load_dwordx4 v[208:211], v1, s[40:43], s28 offen offset:2048 sc1
  buffer_load_dwordx4 v[212:215], v1, s[40:43], s28 offen offset:3072 sc1
  v_lshrrev_b32 v56, 5, v0
  v_mul_u32_u24 v56, 0xc00, v56
  v_and_b32 v57, 31, v0
  v_lshl_add_u32 v56, v57, 3, v56
  s_mul_i32 s55, s46, 0x300
  v_add_u32 v56, s55, v56
  v_add_u32 v57, 0xf000, v56
  v_and_b32 v2, 15, v0
  v_lshlrev_b32 v2, 1, v2
  v_and_b32 v58, 32, v0
  v_add_lshl_u32 v2, v2, v58, 1
  v_add_u32 v58, 0xf000, v57
  s_waitcnt vmcnt(6)
  v_lshlrev_b32 v232, 16, v234
  v_and_b32 v233, 0xffff0000, v234
  v_lshlrev_b32 v234, 16, v235
  v_and_b32 v235, 0xffff0000, v235
  s_waitcnt vmcnt(5)
  v_lshlrev_b32 v236, 16, v238
  v_and_b32 v237, 0xffff0000, v238
  v_lshlrev_b32 v238, 16, v239
  v_and_b32 v239, 0xffff0000, v239
  s_waitcnt lgkmcnt(0)
  s_barrier
  ds_read_b64_tr_b16 v[64:65], v56
  ds_read_b64_tr_b16 v[66:67], v56 offset:6144
  ds_read_b64_tr_b16 v[68:69], v56 offset:256
  ds_read_b64_tr_b16 v[70:71], v56 offset:6400
  ds_read_b64_tr_b16 v[72:73], v56 offset:512
  ds_read_b64_tr_b16 v[74:75], v56 offset:6656
  ds_read_b64_tr_b16 v[76:77], v56 offset:12288
  ds_read_b64_tr_b16 v[78:79], v56 offset:18432
  ds_read_b64_tr_b16 v[80:81], v56 offset:12544
  ds_read_b64_tr_b16 v[82:83], v56 offset:18688
  ds_read_b64_tr_b16 v[84:85], v56 offset:12800
  ds_read_b64_tr_b16 v[86:87], v56 offset:18944
  ds_read_b64_tr_b16 v[88:89], v56 offset:24576
  ds_read_b64_tr_b16 v[90:91], v56 offset:30720
  ds_read_b64_tr_b16 v[92:93], v56 offset:24832
  ds_read_b64_tr_b16 v[94:95], v56 offset:30976
  ds_read_b64_tr_b16 v[96:97], v56 offset:25088
  ds_read_b64_tr_b16 v[98:99], v56 offset:31232
  ds_read_b64_tr_b16 v[100:101], v56 offset:36864
  ds_read_b64_tr_b16 v[102:103], v56 offset:43008
  ds_read_b64_tr_b16 v[104:105], v56 offset:37120
  ds_read_b64_tr_b16 v[106:107], v56 offset:43264
  ds_read_b64_tr_b16 v[108:109], v56 offset:37376
  ds_read_b64_tr_b16 v[110:111], v56 offset:43520
  s_xor_b32 s59, s31, 1
  s_mul_i32 s59, s59, 0x56000
  s_add_u32 s59, s59, s99
  s_add_u32 m0, s51, 0xc000
  s_nop 0
  buffer_load_dwordx4 v7, s[16:19], s59 offen lds
  s_xor_b32 s59, s31, 1
  s_mul_i32 s59, s59, 0x56000
  s_add_u32 s59, s59, s99
  s_add_u32 m0, s51, 0xc380
  s_nop 0
  buffer_load_dwordx4 v7, s[16:19], s59 offen offset:128 lds
  s_xor_b32 s59, s31, 1
  s_mul_i32 s59, s59, 0x56000
  s_add_u32 s59, s59, s99
  s_add_u32 m0, s51, 0xc700
  s_nop 0
  buffer_load_dwordx4 v4, s[16:19], s59 offen offset:256 lds
Lck9_%=:
  s_waitcnt vmcnt(5)
  v_max3_u32 v251, v200, v202, v204
  s_waitcnt vmcnt(4)
  v_max3_u32 v251, v251, v206, v208
  s_waitcnt vmcnt(3)
  v_max3_u32 v251, v251, v210, v212
  v_max_u32 v251, v251, v214
  v_cmp_lt_u32 vcc, s53, v251
  s_cbranch_vccnz Lsl10_%=
  v_mfma_f32_16x16x32_bf16 v[20:23], v[148:151], v[200:203], 0
  v_mfma_f32_16x16x32_bf16 v[16:19], v[144:147], v[200:203], 0
  v_mfma_f32_16x16x32_bf16 v[12:15], v[140:143], v[200:203], 0
  v_mfma_f32_16x16x32_bf16 v[8:11], v[136:139], v[200:203], 0
  v_mfma_f32_16x16x32_bf16 v[20:23], v[164:167], v[204:207], v[20:23]
  v_mfma_f32_16x16x32_bf16 v[16:19], v[160:163], v[204:207], v[16:19]
  v_mfma_f32_16x16x32_bf16 v[12:15], v[156:159], v[204:207], v[12:15]
  v_mfma_f32_16x16x32_bf16 v[8:11], v[152:155], v[204:207], v[8:11]
  buffer_load_dwordx4 v[216:219], v1, s[40:43], s29 offen sc1
  v_mfma_f32_16x16x32_bf16 v[20:23], v[180:183], v[208:211], v[20:23]
  v_mfma_f32_16x16x32_bf16 v[16:19], v[176:179], v[208:211], v[16:19]
  buffer_load_dwordx4 v[220:223], v1, s[40:43], s29 offen offset:1024 sc1
  v_mfma_f32_16x16x32_bf16 v[12:15], v[172:175], v[208:211], v[12:15]
  v_mfma_f32_16x16x32_bf16 v[8:11], v[168:171], v[208:211], v[8:11]
  buffer_load_dwordx4 v[224:227], v1, s[40:43], s29 offen offset:2048 sc1
  v_mfma_f32_16x16x32_bf16 v[20:23], v[196:199], v[212:215], v[20:23]
  v_mfma_f32_16x16x32_bf16 v[16:19], v[192:195], v[212:215], v[16:19]
  buffer_load_dwordx4 v[228:231], v1, s[40:43], s29 offen offset:3072 sc1
  v_mfma_f32_16x16x32_bf16 v[12:15], v[188:191], v[212:215], v[12:15]
  v_mfma_f32_16x16x32_bf16 v[8:11], v[184:187], v[212:215], v[8:11]
  s_nop 3
  ds_write_b128 v254, v[20:23]
  ds_write_b128 v253, v[16:19]
  s_nop 0
  ds_write_b128 v252, v[12:15]
  s_waitcnt lgkmcnt(0)
  s_barrier
  ds_read_b128 v[12:15], v255
  ds_read_b128 v[16:19], v255 offset:1024
  ds_read_b128 v[20:23], v255 offset:2048
  s_xor_b32 s59, s31, 1
  s_mul_i32 s59, s59, 0x56000
  s_add_u32 s59, s59, s99
  s_add_u32 s59, s59, 0x2b000
  s_add_u32 m0, s51, 0x14500
  s_nop 0
  buffer_load_dwordx4 v7, s[16:19], s59 offen lds
  s_xor_b32 s59, s31, 1
  s_mul_i32 s59, s59, 0x56000
  s_add_u32 s59, s59, s99
  s_add_u32 s59, s59, 0x2b000
  s_add_u32 m0, s51, 0x14880
  s_nop 0
  buffer_load_dwordx4 v7, s[16:19], s59 offen offset:128 lds
  s_xor_b32 s59, s31, 1
  s_mul_i32 s59, s59, 0x56000
  s_add_u32 s59, s59, s99
  s_add_u32 s59, s59, 0x2b000
  s_add_u32 m0, s51, 0x14c00
  s_nop 0
  buffer_load_dwordx4 v4, s[16:19], s59 offen offset:256 lds
  s_waitcnt lgkmcnt(2)
  v_pk_add_f32 v[8:9], v[8:9], v[12:13]
  v_pk_add_f32 v[10:11], v[10:11], v[14:15]
  s_waitcnt lgkmcnt(0)
  v_pk_add_f32 v[16:17], v[16:17], v[20:21]
  v_pk_add_f32 v[18:19], v[18:19], v[22:23]
  v_pk_add_f32 v[8:9], v[8:9], v[16:17]
  v_pk_add_f32 v[10:11], v[10:11], v[18:19]
  v_pk_fma_f32 v[232:233], v[240:241], v[8:9], v[232:233]
  v_pk_fma_f32 v[234:235], v[242:243], v[10:11], v[234:235]
  v_pk_mul_f32 v[12:13], v[244:245], v[232:233]
  v_pk_mul_f32 v[14:15], v[246:247], v[234:235]
  v_pk_mul_f32 v[16:17], v[232:233], v[232:233]
  v_pk_mul_f32 v[18:19], v[234:235], v[234:235]
  v_cvt_pk_bf16_f32 v12, v12, v13
  v_cvt_pk_bf16_f32 v13, v14, v15
  v_pk_add_f32 v[20:21], v[232:233], v[234:235]
  v_min_u32 v12, -2, v12
  v_pk_add_f32 v[16:17], v[16:17], v[18:19]
  s_add_u32 s55, s97, 0x18000
  buffer_store_dwordx2 v[12:13], v3, s[40:43], s55 offen sc1
  v_add_f32 v20, v20, v21
  v_add_f32 v16, v16, v17
  s_nop 1
  v_permlane32_swap_b32 v20, v16
  v_add_f32 v20, v20, v16
  v_mov_b32 v16, v20
  s_nop 1
  v_permlane16_swap_b32 v20, v16
  v_add_f32 v20, v20, v16
  v_min_u32 v20, -2, v20
  s_lshr_b32 s55, s97, 2
  s_add_u32 s55, s55, 0x1000
  buffer_store_dword v20, v2, s[40:43], s55 offen sc1
Lck11_%=:
  s_waitcnt vmcnt(7)
  v_max3_u32 v251, v216, v218, v220
  s_waitcnt vmcnt(6)
  v_max3_u32 v251, v251, v222, v224
  s_waitcnt vmcnt(5)
  v_max3_u32 v251, v251, v226, v228
  v_max_u32 v251, v251, v230
  v_cmp_lt_u32 vcc, s53, v251
  s_cbranch_vccnz Lsl12_%=
  s_xor_b32 s59, s31, 2
  s_mul_i32 s59, s59, 0x56000
  s_add_u32 s59, s59, s99
  s_add_u32 m0, s51, 0
  s_nop 0
  buffer_load_dwordx4 v7, s[16:19], s59 offen lds
  v_mfma_f32_16x16x32_bf16 v[36:39], v[148:151], v[216:219], 0
  v_mfma_f32_16x16x32_bf16 v[32:35], v[144:147], v[216:219], 0
  s_xor_b32 s59, s31, 2
  s_mul_i32 s59, s59, 0x56000
  s_add_u32 s59, s59, s99
  s_add_u32 m0, s51, 0x380
  s_nop 0
  buffer_load_dwordx4 v7, s[16:19], s59 offen offset:128 lds
  v_mfma_f32_16x16x32_bf16 v[28:31], v[140:143], v[216:219], 0
  v_mfma_f32_16x16x32_bf16 v[24:27], v[136:139], v[216:219], 0
  v_mfma_f32_16x16x32_bf16 v[36:39], v[164:167], v[220:223], v[36:39]
  s_xor_b32 s59, s31, 2
  s_mul_i32 s59, s59, 0x56000
  s_add_u32 s59, s59, s99
  s_add_u32 m0, s51, 0x700
  s_nop 0
  buffer_load_dwordx4 v4, s[16:19], s59 offen offset:256 lds
  v_mfma_f32_16x16x32_bf16 v[32:35], v[160:163], v[220:223], v[32:35]
  v_mfma_f32_16x16x32_bf16 v[28:31], v[156:159], v[220:223], v[28:31]
  v_mfma_f32_16x16x32_bf16 v[24:27], v[152:155], v[220:223], v[24:27]
  s_xor_b32 s59, s31, 2
  s_mul_i32 s59, s59, 0x56000
  s_add_u32 s59, s59, s99
  s_add_u32 s59, s59, 0x2b000
  s_add_u32 m0, s51, 0x6000
  s_nop 0
  buffer_load_dwordx4 v7, s[16:19], s59 offen lds
  v_mfma_f32_16x16x32_bf16 v[36:39], v[180:183], v[224:227], v[36:39]
  v_mfma_f32_16x16x32_bf16 v[32:35], v[176:179], v[224:227], v[32:35]
  s_xor_b32 s59, s31, 2
  s_mul_i32 s59, s59, 0x56000
  s_add_u32 s59, s59, s99
  s_add_u32 s59, s59, 0x2b000
  s_add_u32 m0, s51, 0x6380
  s_nop 0
  buffer_load_dwordx4 v7, s[16:19], s59 offen offset:128 lds
  v_mfma_f32_16x16x32_bf16 v[28:31], v[172:175], v[224:227], v[28:31]
  v_mfma_f32_16x16x32_bf16 v[24:27], v[168:171], v[224:227], v[24:27]
  v_mfma_f32_16x16x32_bf16 v[36:39], v[196:199], v[228:231], v[36:39]
  s_xor_b32 s59, s31, 2
  s_mul_i32 s59, s59, 0x56000
  s_add_u32 s59, s59, s99
  s_add_u32 s59, s59, 0x2b000
  s_add_u32 m0, s51, 0x6700
  s_nop 0
  buffer_load_dwordx4 v4, s[16:19], s59 offen offset:256 lds
  v_mfma_f32_16x16x32_bf16 v[32:35], v[192:195], v[228:231], v[32:35]
  v_mfma_f32_16x16x32_bf16 v[28:31], v[188:191], v[228:231], v[28:31]
  v_mfma_f32_16x16x32_bf16 v[24:27], v[184:187], v[228:231], v[24:27]
  ds_write_b128 v254, v[36:39] offset:3072
  s_nop 4
  ds_write_b128 v253, v[32:35] offset:3072
  ds_write_b128 v252, v[28:31] offset:3072
  s_waitcnt vmcnt(8)
  s_waitcnt lgkmcnt(0)
  s_barrier
  ds_read_b128 v[28:31], v255 offset:3072
  ds_read_b128 v[32:35], v255 offset:4096
  ds_read_b128 v[36:39], v255 offset:5120
  s_add_u32 s28, s28, 0x10000
  s_add_u32 s29, s29, 0x10000
  buffer_load_dwordx4 v[8:11], v1, s[40:43], s28 offen sc1
  buffer_load_dwordx4 v[12:15], v1, s[40:43], s28 offen offset:1024 sc1
  buffer_load_dwordx4 v[16:19], v1, s[40:43], s28 offen offset:2048 sc1
  buffer_load_dwordx4 v[20:23], v1, s[40:43], s28 offen offset:3072 sc1
  s_waitcnt lgkmcnt(2)
  v_pk_add_f32 v[24:25], v[24:25], v[28:29]
  v_pk_add_f32 v[26:27], v[26:27], v[30:31]
  s_waitcnt lgkmcnt(0)
  v_pk_add_f32 v[32:33], v[32:33], v[36:37]
  v_pk_add_f32 v[34:35], v[34:35], v[38:39]
  v_pk_add_f32 v[24:25], v[24:25], v[32:33]
  v_pk_add_f32 v[26:27], v[26:27], v[34:35]
  v_pk_fma_f32 v[236:237], v[240:241], v[24:25], v[236:237]
  v_pk_fma_f32 v[238:239], v[242:243], v[26:27], v[238:239]
  v_pk_mul_f32 v[28:29], v[244:245], v[236:237]
  v_pk_mul_f32 v[30:31], v[246:247], v[238:239]
  v_pk_mul_f32 v[32:33], v[236:237], v[236:237]
  v_pk_mul_f32 v[34:35], v[238:239], v[238:239]
  v_cvt_pk_bf16_f32 v28, v28, v29
  v_cvt_pk_bf16_f32 v29, v30, v31
  v_pk_add_f32 v[36:37], v[236:237], v[238:239]
  v_min_u32 v28, -2, v28
  v_pk_add_f32 v[32:33], v[32:33], v[34:35]
  s_add_u32 s55, s97, 0x1c000
  buffer_store_dwordx2 v[28:29], v3, s[40:43], s55 offen sc1
  v_add_f32 v36, v36, v37
  v_add_f32 v32, v32, v33
  s_nop 1
  v_permlane32_swap_b32 v36, v32
  v_add_f32 v36, v36, v32
  v_mov_b32 v32, v36
  s_nop 1
  v_permlane16_swap_b32 v36, v32
  v_add_f32 v36, v36, v32
  v_min_u32 v36, -2, v36
  s_lshr_b32 s55, s97, 2
  s_add_u32 s55, s55, 0x2000
  buffer_store_dword v36, v2, s[40:43], s55 offen sc1
  ds_read_b64_tr_b16 v[112:113], v56 offset:49152
  ds_read_b64_tr_b16 v[114:115], v56 offset:55296
  ds_read_b64_tr_b16 v[116:117], v56 offset:49408
  ds_read_b64_tr_b16 v[118:119], v56 offset:55552
  ds_read_b64_tr_b16 v[120:121], v56 offset:49664
  ds_read_b64_tr_b16 v[122:123], v56 offset:55808
  ds_read_b64_tr_b16 v[124:125], v57
  ds_read_b64_tr_b16 v[126:127], v57 offset:6144
  ds_read_b64_tr_b16 v[128:129], v57 offset:256
  ds_read_b64_tr_b16 v[130:131], v57 offset:6400
  ds_read_b64_tr_b16 v[132:133], v57 offset:512
  ds_read_b64_tr_b16 v[134:135], v57 offset:6656
  ds_read_b64_tr_b16 v[136:137], v57 offset:21760
  ds_read_b64_tr_b16 v[138:139], v57 offset:27904
  ds_read_b64_tr_b16 v[140:141], v57 offset:22016
  ds_read_b64_tr_b16 v[142:143], v57 offset:28160
  ds_read_b64_tr_b16 v[144:145], v57 offset:22272
  ds_read_b64_tr_b16 v[146:147], v57 offset:28416
  ds_read_b64_tr_b16 v[148:149], v57 offset:34048
  ds_read_b64_tr_b16 v[150:151], v57 offset:40192
  ds_read_b64_tr_b16 v[152:153], v57 offset:34304
  ds_read_b64_tr_b16 v[154:155], v57 offset:40448
  ds_read_b64_tr_b16 v[156:157], v57 offset:34560
  ds_read_b64_tr_b16 v[158:159], v57 offset:40704
  v_and_b32 v59, 48, v0
  v_and_b32 v60, 1, v0
  v_lshlrev_b32 v60, 10, v60
  v_sub_u32 v59, v59, v60
  v_add_u32 v59, 0x27900, v59
  ds_read_b128 v[240:243], v59
  ds_read_b128 v[244:247], v59 offset:64
  ds_read_b128 v[252:255], v59 offset:128
  ds_read_b128 v[208:211], v59 offset:192
  s_xor_b32 s55, s46, s31
  s_lshl3_add_u32 s55, s55, s45
  s_lshl_b32 s55, s55, 10
  v_add_u32 v62, s55, v1
  v_add_u32 v60, 0x1a500, v62
  s_lshl_b32 s55, s44, 10
  s_add_u32 s55, s55, 0x12000
  v_add_u32 v63, s55, v1
  s_lshl3_add_u32 s55, s45, s46
  s_lshl_b32 s55, s55, 10
  s_add_u32 s75, s55, 0x1000
  s_add_u32 s76, s55, 0x2000
  s_waitcnt lgkmcnt(3)
  v_mfma_f32_16x16x32_bf16 v[220:223], v[64:67], v[240:243], 0
  v_mfma_f32_16x16x32_bf16 v[224:227], v[68:71], v[240:243], 0
  v_mfma_f32_16x16x32_bf16 v[228:231], v[72:75], v[240:243], 0
  ds_read_b128 v[240:243], v59 offset:256
  s_waitcnt lgkmcnt(3)
  v_mfma_f32_16x16x32_bf16 v[220:223], v[76:79], v[244:247], v[220:223]
  v_mfma_f32_16x16x32_bf16 v[224:227], v[80:83], v[244:247], v[224:227]
  v_mfma_f32_16x16x32_bf16 v[228:231], v[84:87], v[244:247], v[228:231]
  ds_read_b128 v[244:247], v59 offset:320
  s_waitcnt lgkmcnt(3)
  v_mfma_f32_16x16x32_bf16 v[220:223], v[88:91], v[252:255], v[220:223]
  v_mfma_f32_16x16x32_bf16 v[224:227], v[92:95], v[252:255], v[224:227]
  v_mfma_f32_16x16x32_bf16 v[228:231], v[96:99], v[252:255], v[228:231]
  ds_read_b128 v[252:255], v59 offset:384
  s_waitcnt lgkmcnt(3)
  v_mfma_f32_16x16x32_bf16 v[220:223], v[100:103], v[208:211], v[220:223]
  v_mfma_f32_16x16x32_bf16 v[224:227], v[104:107], v[208:211], v[224:227]
  v_mfma_f32_16x16x32_bf16 v[228:231], v[108:111], v[208:211], v[228:231]
  ds_read_b128 v[208:211], v59 offset:448
  s_waitcnt lgkmcnt(3)
  v_mfma_f32_16x16x32_bf16 v[220:223], v[112:115], v[240:243], v[220:223]
  v_mfma_f32_16x16x32_bf16 v[224:227], v[116:119], v[240:243], v[224:227]
  v_mfma_f32_16x16x32_bf16 v[228:231], v[120:123], v[240:243], v[228:231]
  ds_read_b128 v[240:243], v59 offset:512
  s_waitcnt lgkmcnt(3)
  v_mfma_f32_16x16x32_bf16 v[220:223], v[124:127], v[244:247], v[220:223]
  v_mfma_f32_16x16x32_bf16 v[224:227], v[128:131], v[244:247], v[224:227]
  v_mfma_f32_16x16x32_bf16 v[228:231], v[132:135], v[244:247], v[228:231]
  ds_read_b128 v[244:247], v59 offset:576
  s_waitcnt vmcnt(6)
  s_waitcnt lgkmcnt(0)
  s_barrier
  ds_read_b64_tr_b16 v[160:161], v56
  ds_read_b64_tr_b16 v[162:163], v56 offset:6144
  ds_read_b64_tr_b16 v[164:165], v56 offset:256
  ds_read_b64_tr_b16 v[166:167], v56 offset:6400
  ds_read_b64_tr_b16 v[168:169], v56 offset:512
  ds_read_b64_tr_b16 v[170:171], v56 offset:6656
  ds_read_b64_tr_b16 v[172:173], v56 offset:12288
  ds_read_b64_tr_b16 v[174:175], v56 offset:18432
  ds_read_b64_tr_b16 v[176:177], v56 offset:12544
  ds_read_b64_tr_b16 v[178:179], v56 offset:18688
  ds_read_b64_tr_b16 v[180:181], v56 offset:12800
  ds_read_b64_tr_b16 v[182:183], v56 offset:18944
  ds_read_b64_tr_b16 v[184:185], v56 offset:24576
  ds_read_b64_tr_b16 v[186:187], v56 offset:30720
  ds_read_b64_tr_b16 v[188:189], v56 offset:24832
  ds_read_b64_tr_b16 v[190:191], v56 offset:30976
  ds_read_b64_tr_b16 v[192:193], v56 offset:25088
  ds_read_b64_tr_b16 v[194:195], v56 offset:31232
  ds_read_b64_tr_b16 v[196:197], v56 offset:36864
  ds_read_b64_tr_b16 v[198:199], v56 offset:43008
  ds_read_b64_tr_b16 v[200:201], v56 offset:37120
  ds_read_b64_tr_b16 v[202:203], v56 offset:43264
  ds_read_b64_tr_b16 v[204:205], v56 offset:37376
  ds_read_b64_tr_b16 v[206:207], v56 offset:43520
  buffer_load_dwordx4 v[24:27], v1, s[40:43], s29 offen sc1
  buffer_load_dwordx4 v[28:31], v1, s[40:43], s29 offen offset:1024 sc1
  buffer_load_dwordx4 v[32:35], v1, s[40:43], s29 offen offset:2048 sc1
  buffer_load_dwordx4 v[36:39], v1, s[40:43], s29 offen offset:3072 sc1
  buffer_load_dwordx4 v[40:43], v1, s[40:43], s75 offen sc1
  buffer_load_dwordx4 v[44:47], v1, s[40:43], s76 offen sc1
  v_mfma_f32_16x16x32_bf16 v[220:223], v[136:139], v[252:255], v[220:223]
  v_mfma_f32_16x16x32_bf16 v[224:227], v[140:143], v[252:255], v[224:227]
  v_mfma_f32_16x16x32_bf16 v[228:231], v[144:147], v[252:255], v[228:231]
  ds_read_b128 v[252:255], v59 offset:640
  v_mfma_f32_16x16x32_bf16 v[220:223], v[148:151], v[208:211], v[220:223]
  v_mfma_f32_16x16x32_bf16 v[224:227], v[152:155], v[208:211], v[224:227]
  v_mfma_f32_16x16x32_bf16 v[228:231], v[156:159], v[208:211], v[228:231]
  ds_read_b128 v[208:211], v59 offset:704
Lck13_%=:
  s_waitcnt vmcnt(10)
  v_max3_u32 v251, v8, v10, v12
  s_waitcnt vmcnt(9)
  v_max3_u32 v251, v251, v14, v16
  s_waitcnt vmcnt(8)
  v_max3_u32 v251, v251, v18, v20
  v_max_u32 v251, v251, v22
  v_cmp_lt_u32 vcc, s53, v251
  s_cbranch_vccnz Lsl14_%=
  ds_write_b128 v60, v[8:11]
  ds_write_b128 v60, v[12:15] offset:2048
  ds_write_b128 v60, v[16:19] offset:4096
  ds_write_b128 v60, v[20:23] offset:6144
  s_xor_b32 s59, s31, 3
  s_mul_i32 s59, s59, 0x56000
  s_add_u32 s59, s59, s99
  s_add_u32 m0, s51, 0xc000
  s_nop 0
  buffer_load_dwordx4 v7, s[16:19], s59 offen lds
  s_xor_b32 s59, s31, 3
  s_mul_i32 s59, s59, 0x56000
  s_add_u32 s59, s59, s99
  s_add_u32 m0, s51, 0xc380
  s_nop 0
  buffer_load_dwordx4 v7, s[16:19], s59 offen offset:128 lds
  s_xor_b32 s59, s31, 3
  s_mul_i32 s59, s59, 0x56000
  s_add_u32 s59, s59, s99
  s_add_u32 m0, s51, 0xc700
  s_nop 0
  buffer_load_dwordx4 v4, s[16:19], s59 offen offset:256 lds
  s_waitcnt lgkmcnt(0)
  s_barrier
  s_xor_b32 s59, s31, 3
  s_mul_i32 s59, s59, 0x56000
  s_add_u32 s59, s59, s99
  s_add_u32 s59, s59, 0x2b000
  s_add_u32 m0, s51, 0x14500
  s_nop 0
  buffer_load_dwordx4 v7, s[16:19], s59 offen lds
  v_mfma_f32_16x16x32_bf16 v[220:223], v[160:163], v[240:243], v[220:223]
  s_xor_b32 s59, s31, 3
  s_mul_i32 s59, s59, 0x56000
  s_add_u32 s59, s59, s99
  s_add_u32 s59, s59, 0x2b000
  s_add_u32 m0, s51, 0x14880
  s_nop 0
  buffer_load_dwordx4 v7, s[16:19], s59 offen offset:128 lds
  v_mfma_f32_16x16x32_bf16 v[224:227], v[164:167], v[240:243], v[224:227]
  v_mfma_f32_16x16x32_bf16 v[228:231], v[168:171], v[240:243], v[228:231]
  s_xor_b32 s59, s31, 3
  s_mul_i32 s59, s59, 0x56000
  s_add_u32 s59, s59, s99
  s_add_u32 s59, s59, 0x2b000
  s_add_u32 m0, s51, 0x14c00
  s_nop 0
  buffer_load_dwordx4 v4, s[16:19], s59 offen offset:256 lds
  ds_read_b128 v[240:243], v59 offset:768
  v_mfma_f32_16x16x32_bf16 v[220:223], v[172:175], v[244:247], v[220:223]
  s_add_u32 m0, s96, 0x8000
  s_cmp_ge_u32 s27, 11
  s_cselect_b32 s59, 0x58000, 0
  s_sub_u32 s59, s79, s59
  s_add_u32 s59, s59, 0
  buffer_load_dwordx4 v5, s[20:23], s59 offen lds
  v_mfma_f32_16x16x32_bf16 v[224:227], v[176:179], v[244:247], v[224:227]
  v_mfma_f32_16x16x32_bf16 v[228:231], v[180:183], v[244:247], v[228:231]
  s_add_u32 m0, s96, 0x8400
  s_cmp_ge_u32 s27, 11
  s_cselect_b32 s59, 0x58000, 0
  s_sub_u32 s59, s79, s59
  s_add_u32 s59, s59, 0x4000
  buffer_load_dwordx4 v5, s[20:23], s59 offen lds
  ds_read_b128 v[244:247], v59 offset:832
  v_mfma_f32_16x16x32_bf16 v[220:223], v[184:187], v[252:255], v[220:223]
  s_add_u32 m0, s96, 0x22500
  s_cmp_ge_u32 s27, 10
  s_cselect_b32 s59, 0x58000, 0
  s_sub_u32 s59, s79, s59
  s_add_u32 s59, s59, 0x8000
  buffer_load_dwordx4 v5, s[20:23], s59 offen lds
  v_mfma_f32_16x16x32_bf16 v[224:227], v[188:191], v[252:255], v[224:227]
  v_mfma_f32_16x16x32_bf16 v[228:231], v[192:195], v[252:255], v[228:231]
  s_add_u32 m0, s96, 0x22900
  s_cmp_ge_u32 s27, 10
  s_cselect_b32 s59, 0x58000, 0
  s_sub_u32 s59, s79, s59
  s_add_u32 s59, s59, 0xc000
  buffer_load_dwordx4 v5, s[20:23], s59 offen lds
  ds_read_b128 v[252:255], v59 offset:896
  v_mfma_f32_16x16x32_bf16 v[220:223], v[196:199], v[208:211], v[220:223]
  v_mfma_f32_16x16x32_bf16 v[224:227], v[200:203], v[208:211], v[224:227]
  v_mfma_f32_16x16x32_bf16 v[228:231], v[204:207], v[208:211], v[228:231]
  ds_read_b128 v[208:211], v59 offset:960
  s_lshl3_add_u32 s56, s2, s44
  s_cmp_lg_u32 s56, 7
  s_cbranch_scc1 Lsk15_%=
  v_lshrrev_b32 v12, 3, v0
  v_mul_u32_u24 v12, 0x54000, v12
  v_and_b32 v13, 7, v0
  v_lshl_add_u32 v12, v13, 6, v12
  s_mov_b32 s55, 0x10000
  s_mov_b32 s56, 0x2b0000
  s_mov_b32 s57, 0x550000
  s_mov_b32 s77, 0x7f0000
  buffer_load_dword v8, v12, s[36:39], s55 offen sc1
  buffer_load_dword v9, v12, s[36:39], s56 offen sc1
  buffer_load_dword v10, v12, s[36:39], s57 offen sc1
  buffer_load_dword v11, v12, s[36:39], s77 offen sc1
Lck16_%=:
  s_waitcnt vmcnt(3)
  v_cmp_ne_u32 vcc, s49, v8
  s_waitcnt vmcnt(2)
  v_cmp_ne_u32 s[58:59], s49, v9
  s_or_b64 vcc, vcc, s[58:59]
  s_waitcnt vmcnt(1)
  v_cmp_ne_u32 s[58:59], s49, v10
  s_or_b64 vcc, vcc, s[58:59]
  s_waitcnt vmcnt(0)
  v_cmp_ne_u32 s[58:59], s49, v11
  s_or_b64 vcc, vcc, s[58:59]
  s_and_b64 vcc, vcc, s[0:1]
  s_cbranch_scc1 Lsl17_%=
  v_mov_b32 v8, s49
  s_mov_b64 exec, 1
  buffer_store_dword v8, off, s[36:39], 0 sc1
  s_mov_b64 exec, -1
Lsk15_%=:
Lck18_%=:
  s_waitcnt vmcnt(14)
  v_max3_u32 v251, v24, v26, v28
  s_waitcnt vmcnt(13)
  v_max3_u32 v251, v251, v30, v32
  s_waitcnt vmcnt(12)
  v_max3_u32 v251, v251, v34, v36
  s_waitcnt vmcnt(11)
  v_max3_u32 v251, v251, v38, v40
  v_max3_u32 v251, v251, v41, v42
  s_waitcnt vmcnt(10)
  v_max3_u32 v251, v251, v43, v44
  v_max3_u32 v251, v251, v45, v46
  v_max_u32 v251, v251, v47
  v_cmp_lt_u32 vcc, s53, v251
  s_cbranch_vccnz Lsl19_%=
  ds_write_b128 v62, v[24:27]
  ds_write_b128 v62, v[28:31] offset:2048
  ds_write_b128 v62, v[32:35] offset:4096
  ds_write_b128 v62, v[36:39] offset:6144
  v_permlane32_swap_b32 v40, v44
  v_permlane32_swap_b32 v41, v45
  v_permlane32_swap_b32 v42, v46
  v_permlane32_swap_b32 v43, v47
  v_pk_add_f32 v[40:41], v[40:41], v[44:45]
  v_pk_add_f32 v[42:43], v[42:43], v[46:47]
  s_nop 0
  v_add_f32_dpp v40, v40, v40 row_ror:8 row_mask:0xf bank_mask:0xf
  v_add_f32_dpp v41, v41, v41 row_ror:8 row_mask:0xf bank_mask:0xf
  v_add_f32_dpp v42, v42, v42 row_ror:8 row_mask:0xf bank_mask:0xf
  v_add_f32_dpp v43, v43, v43 row_ror:8 row_mask:0xf bank_mask:0xf
  ds_write_b128 v63, v[40:43]
  s_lshl_b32 s55, s45, 10
  v_add_u32 v61, s55, v1
  v_add_u32 v60, 0x1a500, v61
  s_waitcnt lgkmcnt(0)
  s_barrier
  ds_read_b128 v[36:39], v61
  ds_read_b128 v[32:35], v60
  ds_read_b128 v[44:47], v61 offset:2048
  ds_read_b128 v[40:43], v60 offset:2048
  s_waitcnt lgkmcnt(3)
  v_mfma_f32_16x16x32_bf16 v[12:15], v[64:67], v[36:39], 0
  v_subrev_u32 v4, 0x14500, v249
  v_subrev_u32 v7, 0x14500, v250
  ds_read_b128 v[52:55], v61 offset:4096
  ds_read_b128 v[48:51], v60 offset:4096
  v_mfma_f32_16x16x32_bf16 v[20:23], v[68:71], v[36:39], 0
  v_and_b32 v251, 15, v0
  v_mfma_f32_16x16x32_bf16 v[28:31], v[72:75], v[36:39], 0
  v_lshrrev_b32 v62, 4, v0
  s_waitcnt lgkmcnt(4)
  v_mfma_f32_16x16x32_bf16 v[8:11], v[64:67], v[32:35], 0
  s_lshl_b32 s55, s45, 12
  s_add_u32 s55, s55, 0x12000
  v_lshl_add_u32 v62, v62, 10, s55
  v_mfma_f32_16x16x32_bf16 v[16:19], v[68:71], v[32:35], 0
  v_lshl_add_u32 v251, v251, 2, v62
  v_mfma_f32_16x16x32_bf16 v[24:27], v[72:75], v[32:35], 0
  ds_read2_b32 v[212:213], v251 offset0:0 offset1:16
  s_waitcnt lgkmcnt(4)
  v_mfma_f32_16x16x32_bf16 v[12:15], v[76:79], v[44:47], v[12:15]
  ds_read2_b32 v[214:215], v251 offset0:64 offset1:80
  ds_read_b128 v[36:39], v61 offset:6144
  ds_read_b128 v[32:35], v60 offset:6144
  v_mfma_f32_16x16x32_bf16 v[20:23], v[80:83], v[44:47], v[20:23]
  ds_read2_b32 v[216:217], v251 offset0:128 offset1:144
  v_mfma_f32_16x16x32_bf16 v[28:31], v[84:87], v[44:47], v[28:31]
  ds_read2_b32 v[218:219], v251 offset0:192 offset1:208
  s_waitcnt lgkmcnt(4)
  v_add_f32 v212, v212, v214
  v_mfma_f32_16x16x32_bf16 v[8:11], v[76:79], v[40:43], v[8:11]
  v_add_f32 v213, v213, v215
  v_mfma_f32_16x16x32_bf16 v[16:19], v[80:83], v[40:43], v[16:19]
  s_waitcnt lgkmcnt(0)
  v_add_f32 v216, v216, v218
  v_mfma_f32_16x16x32_bf16 v[24:27], v[84:87], v[40:43], v[24:27]
  v_add_f32 v217, v217, v219
  v_mfma_f32_16x16x32_bf16 v[12:15], v[88:91], v[52:55], v[12:15]
  v_permlane32_swap_b32 v212, v213
  ds_read_b128 v[44:47], v61 offset:8192
  ds_read_b128 v[40:43], v60 offset:8192
  v_mfma_f32_16x16x32_bf16 v[20:23], v[92:95], v[52:55], v[20:23]
  s_waitcnt vmcnt(4)
  s_barrier
  ds_read_b64_tr_b16 v[64:65], v56 offset:49152
  v_permlane32_swap_b32 v216, v217
  v_mfma_f32_16x16x32_bf16 v[28:31], v[96:99], v[52:55], v[28:31]
  ds_read_b64_tr_b16 v[66:67], v56 offset:55296
  v_add_f32 v212, v212, v213
  v_mfma_f32_16x16x32_bf16 v[8:11], v[88:91], v[48:51], v[8:11]
  ds_read_b64_tr_b16 v[68:69], v56 offset:49408
  v_add_f32 v216, v216, v217
  v_mfma_f32_16x16x32_bf16 v[16:19], v[92:95], v[48:51], v[16:19]
  ds_read_b64_tr_b16 v[70:71], v56 offset:55552
  v_permlane16_swap_b32 v212, v216
  v_mfma_f32_16x16x32_bf16 v[24:27], v[96:99], v[48:51], v[24:27]
  ds_read_b64_tr_b16 v[72:73], v56 offset:49664
  ds_read_b64_tr_b16 v[74:75], v56 offset:55808
  v_add_f32 v212, v212, v216
  v_mov_b32 v214, v212
  v_mfma_f32_16x16x32_bf16 v[12:15], v[100:103], v[36:39], v[12:15]
  ds_read_b64_tr_b16 v[76:77], v57
  v_permlane32_swap_b32 v212, v214
  ds_read_b128 v[52:55], v61 offset:10240
  ds_read_b128 v[48:51], v60 offset:10240
  v_mfma_f32_16x16x32_bf16 v[20:23], v[104:107], v[36:39], v[20:23]
  ds_read_b64_tr_b16 v[78:79], v57 offset:6144
  v_mul_f32 v212, 0xbb000000, v212
  v_mfma_f32_16x16x32_bf16 v[28:31], v[108:111], v[36:39], v[28:31]
  ds_read_b64_tr_b16 v[80:81], v57 offset:256
  v_mul_f32 v214, 0x3b000000, v214
  v_mfma_f32_16x16x32_bf16 v[8:11], v[100:103], v[32:35], v[8:11]
  ds_read_b64_tr_b16 v[82:83], v57 offset:6400
  ds_read_b64_tr_b16 v[84:85], v57 offset:512
  v_fma_f32 v214, -v212, v212, v214
  v_mfma_f32_16x16x32_bf16 v[16:19], v[104:107], v[32:35], v[16:19]
  ds_read_b64_tr_b16 v[86:87], v57 offset:6656
  v_max_f32 v214, 0, v214
  v_mfma_f32_16x16x32_bf16 v[24:27], v[108:111], v[32:35], v[24:27]
  ds_read_b64_tr_b16 v[88:89], v57 offset:21760
  v_add_f32 v214, 0x3727c5ac, v214
  s_waitcnt lgkmcnt(15)
  v_mfma_f32_16x16x32_bf16 v[12:15], v[112:115], v[44:47], v[12:15]
  ds_read_b64_tr_b16 v[90:91], v57 offset:27904
  v_rsq_f32 v214, v214
  ds_read_b128 v[36:39], v61 offset:12288
  ds_read_b128 v[32:35], v60 offset:12288
  v_mfma_f32_16x16x32_bf16 v[20:23], v[116:119], v[44:47], v[20:23]
  ds_read_b64_tr_b16 v[92:93], v57 offset:22016
  ds_read_b64_tr_b16 v[94:95], v57 offset:28160
  v_mul_f32 v212, v212, v214
  v_mul_f32 v214, 0xbfb8aa3b, v214
  v_mfma_f32_16x16x32_bf16 v[28:31], v[120:123], v[44:47], v[28:31]
  ds_read_b64_tr_b16 v[96:97], v57 offset:22272
  v_mov_b32 v213, v212
  s_waitcnt lgkmcnt(15)
  v_mfma_f32_16x16x32_bf16 v[8:11], v[112:115], v[40:43], v[8:11]
  ds_read_b64_tr_b16 v[98:99], v57 offset:28416
  v_mov_b32 v215, v214
  v_mfma_f32_16x16x32_bf16 v[16:19], v[116:119], v[40:43], v[16:19]
  ds_read_b64_tr_b16 v[100:101], v57 offset:34048
  v_permlane16_swap_b32 v212, v213
  v_mfma_f32_16x16x32_bf16 v[24:27], v[120:123], v[40:43], v[24:27]
  ds_read_b64_tr_b16 v[102:103], v57 offset:40192
  ds_read_b64_tr_b16 v[104:105], v57 offset:34304
  v_permlane16_swap_b32 v214, v215
  s_waitcnt lgkmcnt(15)
  v_mfma_f32_16x16x32_bf16 v[12:15], v[124:127], v[52:55], v[12:15]
  ds_read_b64_tr_b16 v[106:107], v57 offset:40448
  v_mov_b32 v216, v212
  ds_read_b128 v[44:47], v61 offset:14336
  ds_read_b128 v[40:43], v60 offset:14336
  v_mfma_f32_16x16x32_bf16 v[20:23], v[128:131], v[52:55], v[20:23]
  ds_read_b64_tr_b16 v[108:109], v57 offset:34560
  v_mov_b32 v218, v214
  v_mfma_f32_16x16x32_bf16 v[28:31], v[132:135], v[52:55], v[28:31]
  ds_read_b64_tr_b16 v[110:111], v57 offset:40704
  v_mov_b32 v217, v213
  s_waitcnt lgkmcnt(15)
  v_mfma_f32_16x16x32_bf16 v[8:11], v[124:127], v[48:51], v[8:11]
  v_mov_b32 v219, v215
  v_mfma_f32_16x16x32_bf16 v[16:19], v[128:131], v[48:51], v[16:19]
  v_mfma_f32_16x16x32_bf16 v[24:27], v[132:135], v[48:51], v[24:27]
  s_waitcnt vmcnt(0)
  s_waitcnt lgkmcnt(0)
  s_barrier
  s_add_u32 m0, s96, 0xc000
  s_cmp_ge_u32 s27, 9
  s_cselect_b32 s59, 0x58000, 0
  s_sub_u32 s59, s79, s59
  s_add_u32 s59, s59, 0x10000
  buffer_load_dwordx4 v5, s[20:23], s59 offen lds
  ds_read_b64_tr_b16 v[124:125], v7 offset:33280
  v_mfma_f32_16x16x32_bf16 v[220:223], v[64:67], v[240:243], v[220:223]
  ds_read_b64_tr_b16 v[126:127], v7 offset:35328
  v_mfma_f32_16x16x32_bf16 v[224:227], v[68:71], v[240:243], v[224:227]
  ds_read_b64_tr_b16 v[120:121], v4 offset:33280
  v_mfma_f32_16x16x32_bf16 v[228:231], v[72:75], v[240:243], v[228:231]
  s_add_u32 m0, s96, 0xc400
  s_cmp_ge_u32 s27, 9
  s_cselect_b32 s59, 0x58000, 0
  s_sub_u32 s59, s79, s59
  s_add_u32 s59, s59, 0x14000
  buffer_load_dwordx4 v5, s[20:23], s59 offen lds
  v_mfma_f32_16x16x32_bf16 v[220:223], v[76:79], v[244:247], v[220:223]
  ds_read_b64_tr_b16 v[122:123], v4 offset:35328
  v_mfma_f32_16x16x32_bf16 v[224:227], v[80:83], v[244:247], v[224:227]
  ds_read_b64_tr_b16 v[116:117], v7 offset:32768
  v_mfma_f32_16x16x32_bf16 v[228:231], v[84:87], v[244:247], v[228:231]
  s_add_u32 m0, s96, 0x14500
  s_cmp_ge_u32 s27, 8
  s_cselect_b32 s59, 0x58000, 0
  s_sub_u32 s59, s79, s59
  s_add_u32 s59, s59, 0x18000
  buffer_load_dwordx4 v5, s[20:23], s59 offen lds
  ds_read_b64_tr_b16 v[118:119], v7 offset:34816
  v_mfma_f32_16x16x32_bf16 v[220:223], v[88:91], v[252:255], v[220:223]
  v_mfma_f32_16x16x32_bf16 v[224:227], v[92:95], v[252:255], v[224:227]
  ds_read_b64_tr_b16 v[112:113], v4 offset:32768
  v_mfma_f32_16x16x32_bf16 v[228:231], v[96:99], v[252:255], v[228:231]
  s_add_u32 m0, s96, 0x14900
  s_cmp_ge_u32 s27, 8
  s_cselect_b32 s59, 0x58000, 0
  s_sub_u32 s59, s79, s59
  s_add_u32 s59, s59, 0x1c000
  buffer_load_dwordx4 v5, s[20:23], s59 offen lds
  ds_read_b64_tr_b16 v[114:115], v4 offset:34816
  v_mfma_f32_16x16x32_bf16 v[220:223], v[100:103], v[208:211], v[220:223]
  v_mfma_f32_16x16x32_bf16 v[224:227], v[104:107], v[208:211], v[224:227]
  v_mfma_f32_16x16x32_bf16 v[228:231], v[108:111], v[208:211], v[228:231]
  v_mfma_f32_16x16x32_bf16 v[12:15], v[136:139], v[36:39], v[12:15]
  ds_read_b128 v[52:55], v61 offset:16384
  ds_read_b128 v[48:51], v60 offset:16384
  v_mfma_f32_16x16x32_bf16 v[20:23], v[140:143], v[36:39], v[20:23]
  v_mfma_f32_16x16x32_bf16 v[28:31], v[144:147], v[36:39], v[28:31]
  v_mfma_f32_16x16x32_bf16 v[8:11], v[136:139], v[32:35], v[8:11]
  v_mfma_f32_16x16x32_bf16 v[16:19], v[140:143], v[32:35], v[16:19]
  v_mfma_f32_16x16x32_bf16 v[24:27], v[144:147], v[32:35], v[24:27]
  ds_read_b64_tr_b16 v[140:141], v250 offset:57856
  v_mfma_f32_16x16x32_bf16 v[12:15], v[148:151], v[44:47], v[12:15]
  ds_read_b64_tr_b16 v[142:143], v250 offset:59904
  ds_read_b128 v[36:39], v61 offset:18432
  ds_read_b128 v[32:35], v60 offset:18432
  v_mfma_f32_16x16x32_bf16 v[20:23], v[152:155], v[44:47], v[20:23]
  ds_read_b64_tr_b16 v[136:137], v249 offset:57856
  v_mfma_f32_16x16x32_bf16 v[28:31], v[156:159], v[44:47], v[28:31]
  ds_read_b64_tr_b16 v[138:139], v249 offset:59904
  v_mfma_f32_16x16x32_bf16 v[8:11], v[148:151], v[40:43], v[8:11]
  ds_read_b64_tr_b16 v[132:133], v250 offset:57344
  v_mfma_f32_16x16x32_bf16 v[16:19], v[152:155], v[40:43], v[16:19]
  ds_read_b64_tr_b16 v[134:135], v250 offset:59392
  v_mfma_f32_16x16x32_bf16 v[24:27], v[156:159], v[40:43], v[24:27]
  ds_read_b64_tr_b16 v[128:129], v249 offset:57344
  s_waitcnt lgkmcnt(10)
  v_mfma_f32_16x16x32_bf16 v[12:15], v[160:163], v[52:55], v[12:15]
  ds_read_b64_tr_b16 v[130:131], v249 offset:59392
  ds_read_b128 v[44:47], v61 offset:20480
  ds_read_b128 v[40:43], v60 offset:20480
  v_mfma_f32_16x16x32_bf16 v[20:23], v[164:167], v[52:55], v[20:23]
  v_mfma_f32_16x16x32_bf16 v[28:31], v[168:171], v[52:55], v[28:31]
  s_waitcnt lgkmcnt(12)
  v_mfma_f32_16x16x32_bf16 v[8:11], v[160:163], v[48:51], v[8:11]
  v_mfma_f32_16x16x32_bf16 v[16:19], v[164:167], v[48:51], v[16:19]
  v_mfma_f32_16x16x32_bf16 v[24:27], v[168:171], v[48:51], v[24:27]
  s_waitcnt lgkmcnt(0)
  s_barrier
  s_add_u32 m0, s96, 0x8000
  s_cmp_ge_u32 s27, 7
  s_cselect_b32 s59, 0x58000, 0
  s_sub_u32 s59, s79, s59
  s_add_u32 s59, s59, 0x20000
  buffer_load_dwordx4 v5, s[20:23], s59 offen lds
  v_mfma_f32_16x16x32_bf16 v[12:15], v[172:175], v[36:39], v[12:15]
  s_mov_b32 s72, 0xbfb8aa3b
  v_mul_f32 v220, s72, v220
  v_mul_f32 v221, s72, v221
  v_mul_f32 v222, s72, v222
  ds_read_b128 v[52:55], v61 offset:22528
  ds_read_b128 v[48:51], v60 offset:22528
  v_mfma_f32_16x16x32_bf16 v[20:23], v[176:179], v[36:39], v[20:23]
  s_add_u32 m0, s96, 0x8400
  s_cmp_ge_u32 s27, 7
  s_cselect_b32 s59, 0x58000, 0
  s_sub_u32 s59, s79, s59
  s_add_u32 s59, s59, 0x24000
  buffer_load_dwordx4 v5, s[20:23], s59 offen lds
  v_mul_f32 v223, s72, v223
  v_mul_f32 v224, s72, v224
  v_mul_f32 v225, s72, v225
  v_mfma_f32_16x16x32_bf16 v[28:31], v[180:183], v[36:39], v[28:31]
  v_mul_f32 v226, s72, v226
  v_mul_f32 v227, s72, v227
  v_mfma_f32_16x16x32_bf16 v[8:11], v[172:175], v[32:35], v[8:11]
  v_mul_f32 v228, s72, v228
  v_mul_f32 v229, s72, v229
  v_mul_f32 v230, s72, v230
  v_mfma_f32_16x16x32_bf16 v[16:19], v[176:179], v[32:35], v[16:19]
  s_add_u32 m0, s96, 0x22500
  s_cmp_ge_u32 s27, 6
  s_cselect_b32 s59, 0x58000, 0
  s_sub_u32 s59, s79, s59
  s_add_u32 s59, s59, 0x28000
  buffer_load_dwordx4 v5, s[20:23], s59 offen lds
  v_mul_f32 v231, s72, v231
  v_mov_b32_dpp v144, v220 quad_perm:[1,1,3,3] row_mask:0xf bank_mask:0xf
  v_mov_b32_dpp v145, v221 quad_perm:[1,1,3,3] row_mask:0xf bank_mask:0xf
  v_mfma_f32_16x16x32_bf16 v[24:27], v[180:183], v[32:35], v[24:27]
  v_mov_b32_dpp v146, v222 quad_perm:[1,1,3,3] row_mask:0xf bank_mask:0xf
  v_mov_b32_dpp v147, v223 quad_perm:[1,1,3,3] row_mask:0xf bank_mask:0xf
  v_mfma_f32_16x16x32_bf16 v[12:15], v[184:187], v[44:47], v[12:15]
  v_fmac_f32_dpp v144, v220, v216 quad_perm:[0,0,2,2] row_mask:0xf bank_mask:0xf
  v_fmac_f32_dpp v145, v221, v216 quad_perm:[0,0,2,2] row_mask:0xf bank_mask:0xf
  v_fmac_f32_dpp v146, v222, v216 quad_perm:[0,0,2,2] row_mask:0xf bank_mask:0xf
  ds_read_b128 v[36:39], v61 offset:24576
  ds_read_b128 v[32:35], v60 offset:24576
  v_mfma_f32_16x16x32_bf16 v[20:23], v[188:191], v[44:47], v[20:23]
  s_add_u32 m0, s96, 0x22900
  s_cmp_ge_u32 s27, 6
  s_cselect_b32 s59, 0x58000, 0
  s_sub_u32 s59, s79, s59
  s_add_u32 s59, s59, 0x2c000
  buffer_load_dwordx4 v5, s[20:23], s59 offen lds
  v_fmac_f32_dpp v147, v223, v216 quad_perm:[0,0,2,2] row_mask:0xf bank_mask:0xf
  v_mov_b32_dpp v148, v220 quad_perm:[1,1,3,3] row_mask:0xf bank_mask:0xf
  v_mfma_f32_16x16x32_bf16 v[28:31], v[192:195], v[44:47], v[28:31]
  v_mov_b32_dpp v149, v221 quad_perm:[1,1,3,3] row_mask:0xf bank_mask:0xf
  v_mov_b32_dpp v150, v222 quad_perm:[1,1,3,3] row_mask:0xf bank_mask:0xf
  v_mov_b32_dpp v151, v223 quad_perm:[1,1,3,3] row_mask:0xf bank_mask:0xf
  v_mfma_f32_16x16x32_bf16 v[8:11], v[184:187], v[40:43], v[8:11]
  v_fmac_f32_dpp v148, v220, v217 quad_perm:[0,0,2,2] row_mask:0xf bank_mask:0xf
  v_fmac_f32_dpp v149, v221, v217 quad_perm:[0,0,2,2] row_mask:0xf bank_mask:0xf
  v_fmac_f32_dpp v150, v222, v217 quad_perm:[0,0,2,2] row_mask:0xf bank_mask:0xf
  v_mfma_f32_16x16x32_bf16 v[16:19], v[188:191], v[40:43], v[16:19]
  s_add_u32 m0, s96, 0
  s_cmp_ge_u32 s27, 5
  s_cselect_b32 s59, 0x58000, 0
  s_sub_u32 s59, s79, s59
  s_add_u32 s59, s59, 0x30000
  buffer_load_dwordx4 v5, s[20:23], s59 offen lds
  v_fmac_f32_dpp v151, v223, v217 quad_perm:[0,0,2,2] row_mask:0xf bank_mask:0xf
  v_mov_b32_dpp v152, v224 quad_perm:[1,1,3,3] row_mask:0xf bank_mask:0xf
  v_mfma_f32_16x16x32_bf16 v[24:27], v[192:195], v[40:43], v[24:27]
  v_mov_b32_dpp v153, v225 quad_perm:[1,1,3,3] row_mask:0xf bank_mask:0xf
  v_mov_b32_dpp v154, v226 quad_perm:[1,1,3,3] row_mask:0xf bank_mask:0xf
  v_mov_b32_dpp v155, v227 quad_perm:[1,1,3,3] row_mask:0xf bank_mask:0xf
  s_waitcnt lgkmcnt(3)
  v_mfma_f32_16x16x32_bf16 v[12:15], v[196:199], v[52:55], v[12:15]
  v_fmac_f32_dpp v152, v224, v216 quad_perm:[0,0,2,2] row_mask:0xf bank_mask:0xf
  v_fmac_f32_dpp v153, v225, v216 quad_perm:[0,0,2,2] row_mask:0xf bank_mask:0xf
  ds_read_b128 v[44:47], v61 offset:26624
  ds_read_b128 v[40:43], v60 offset:26624
  v_mfma_f32_16x16x32_bf16 v[20:23], v[200:203], v[52:55], v[20:23]
  s_add_u32 m0, s96, 0x400
  s_cmp_ge_u32 s27, 5
  s_cselect_b32 s59, 0x58000, 0
  s_sub_u32 s59, s79, s59
  s_add_u32 s59, s59, 0x34000
  buffer_load_dwordx4 v5, s[20:23], s59 offen lds
  v_fmac_f32_dpp v154, v226, v216 quad_perm:[0,0,2,2] row_mask:0xf bank_mask:0xf
  v_fmac_f32_dpp v155, v227, v216 quad_perm:[0,0,2,2] row_mask:0xf bank_mask:0xf
  v_mov_b32_dpp v156, v224 quad_perm:[1,1,3,3] row_mask:0xf bank_mask:0xf
  v_mfma_f32_16x16x32_bf16 v[28:31], v[204:207], v[52:55], v[28:31]
  v_mov_b32_dpp v157, v225 quad_perm:[1,1,3,3] row_mask:0xf bank_mask:0xf
  v_mov_b32_dpp v158, v226 quad_perm:[1,1,3,3] row_mask:0xf bank_mask:0xf
  v_mov_b32_dpp v159, v227 quad_perm:[1,1,3,3] row_mask:0xf bank_mask:0xf
  s_waitcnt lgkmcnt(4)
  v_mfma_f32_16x16x32_bf16 v[8:11], v[196:199], v[48:51], v[8:11]
  v_fmac_f32_dpp v156, v224, v217 quad_perm:[0,0,2,2] row_mask:0xf bank_mask:0xf
  v_fmac_f32_dpp v157, v225, v217 quad_perm:[0,0,2,2] row_mask:0xf bank_mask:0xf
  v_mfma_f32_16x16x32_bf16 v[16:19], v[200:203], v[48:51], v[16:19]
  s_add_u32 m0, s96, 0x1a500
  s_cmp_ge_u32 s27, 4
  s_cselect_b32 s59, 0x58000, 0
  s_sub_u32 s59, s79, s59
  s_add_u32 s59, s59, 0x38000
  buffer_load_dwordx4 v5, s[20:23], s59 offen lds
  v_fmac_f32_dpp v158, v226, v217 quad_perm:[0,0,2,2] row_mask:0xf bank_mask:0xf
  v_fmac_f32_dpp v159, v227, v217 quad_perm:[0,0,2,2] row_mask:0xf bank_mask:0xf
  v_mov_b32_dpp v160, v228 quad_perm:[1,1,3,3] row_mask:0xf bank_mask:0xf
  v_mfma_f32_16x16x32_bf16 v[24:27], v[204:207], v[48:51], v[24:27]
  v_mov_b32_dpp v161, v229 quad_perm:[1,1,3,3] row_mask:0xf bank_mask:0xf
  v_mov_b32_dpp v162, v230 quad_perm:[1,1,3,3] row_mask:0xf bank_mask:0xf
  s_waitcnt lgkmcnt(3)
  v_mfma_f32_16x16x32_bf16 v[12:15], v[64:67], v[36:39], v[12:15]
  v_mov_b32_dpp v163, v231 quad_perm:[1,1,3,3] row_mask:0xf bank_mask:0xf
  v_fmac_f32_dpp v160, v228, v216 quad_perm:[0,0,2,2] row_mask:0xf bank_mask:0xf
  v_fmac_f32_dpp v161, v229, v216 quad_perm:[0,0,2,2] row_mask:0xf bank_mask:0xf
  ds_read_b128 v[52:55], v61 offset:28672
  ds_read_b128 v[48:51], v60 offset:28672
  v_mfma_f32_16x16x32_bf16 v[20:23], v[68:71], v[36:39], v[20:23]
  s_add_u32 m0, s96, 0x1a900
  s_cmp_ge_u32 s27, 4
  s_cselect_b32 s59, 0x58000, 0
  s_sub_u32 s59, s79, s59
  s_add_u32 s59, s59, 0x3c000
  buffer_load_dwordx4 v5, s[20:23], s59 offen lds
  v_fmac_f32_dpp v162, v230, v216 quad_perm:[0,0,2,2] row_mask:0xf bank_mask:0xf
  v_fmac_f32_dpp v163, v231, v216 quad_perm:[0,0,2,2] row_mask:0xf bank_mask:0xf
  v_mov_b32_dpp v164, v228 quad_perm:[1,1,3,3] row_mask:0xf bank_mask:0xf
  v_mfma_f32_16x16x32_bf16 v[28:31], v[72:75], v[36:39], v[28:31]
  v_mov_b32_dpp v165, v229 quad_perm:[1,1,3,3] row_mask:0xf bank_mask:0xf
  v_mov_b32_dpp v166, v230 quad_perm:[1,1,3,3] row_mask:0xf bank_mask:0xf
  s_waitcnt lgkmcnt(4)
  v_mfma_f32_16x16x32_bf16 v[8:11], v[64:67], v[32:35], v[8:11]
  v_mov_b32_dpp v167, v231 quad_perm:[1,1,3,3] row_mask:0xf bank_mask:0xf
  v_fmac_f32_dpp v164, v228, v217 quad_perm:[0,0,2,2] row_mask:0xf bank_mask:0xf
  v_fmac_f32_dpp v165, v229, v217 quad_perm:[0,0,2,2] row_mask:0xf bank_mask:0xf
  v_mfma_f32_16x16x32_bf16 v[16:19], v[68:71], v[32:35], v[16:19]
  v_fmac_f32_dpp v166, v230, v217 quad_perm:[0,0,2,2] row_mask:0xf bank_mask:0xf
  v_fmac_f32_dpp v167, v231, v217 quad_perm:[0,0,2,2] row_mask:0xf bank_mask:0xf
  v_mfma_f32_16x16x32_bf16 v[24:27], v[72:75], v[32:35], v[24:27]
  s_waitcnt vmcnt(8)
  s_barrier
  ds_read_b128 v[36:39], v61 offset:30720
  ds_read_b128 v[32:35], v60 offset:30720
  ds_read_b64_tr_b16 v[180:181], v7 offset:49664
  ds_read_b64_tr_b16 v[182:183], v7 offset:51712
  s_waitcnt lgkmcnt(7)
  v_mfma_f32_16x16x32_bf16 v[12:15], v[76:79], v[44:47], v[12:15]
  ds_read_b64_tr_b16 v[176:177], v4 offset:49664
  ds_read_b64_tr_b16 v[178:179], v4 offset:51712
  s_waitcnt lgkmcnt(8)
  v_mfma_f32_16x16x32_bf16 v[8:11], v[76:79], v[40:43], v[8:11]
  ds_read_b64_tr_b16 v[172:173], v7 offset:49152
  ds_read_b64_tr_b16 v[174:175], v7 offset:51200
  s_waitcnt lgkmcnt(9)
  v_mfma_f32_16x16x32_bf16 v[12:15], v[88:91], v[52:55], v[12:15]
  ds_read_b64_tr_b16 v[168:169], v4 offset:49152
  ds_read_b64_tr_b16 v[170:171], v4 offset:51200
  s_waitcnt lgkmcnt(10)
  v_mfma_f32_16x16x32_bf16 v[8:11], v[88:91], v[48:51], v[8:11]
  ds_read_b64_tr_b16 v[196:197], v250 offset:512
  s_waitcnt lgkmcnt(10)
  v_mfma_f32_16x16x32_bf16 v[12:15], v[100:103], v[36:39], v[12:15]
  ds_read_b64_tr_b16 v[198:199], v250 offset:2560
  ds_read_b64_tr_b16 v[192:193], v249 offset:512
  s_waitcnt lgkmcnt(11)
  v_mfma_f32_16x16x32_bf16 v[8:11], v[100:103], v[32:35], v[8:11]
  ds_read_b64_tr_b16 v[194:195], v249 offset:2560
  ds_read_b64_tr_b16 v[188:189], v250
  v_mfma_f32_16x16x32_bf16 v[20:23], v[80:83], v[44:47], v[20:23]
  ds_read_b64_tr_b16 v[190:191], v250 offset:2048
  ds_read_b64_tr_b16 v[184:185], v249
  v_mfma_f32_16x16x32_bf16 v[16:19], v[80:83], v[40:43], v[16:19]
  ds_read_b64_tr_b16 v[186:187], v249 offset:2048
  s_nop 0
  v_fma_f32 v8, v8, v218, v144
  v_fma_f32 v9, v9, v218, v145
  v_fma_f32 v10, v10, v218, v146
  v_fma_f32 v11, v11, v218, v147
  v_fma_f32 v12, v12, v219, v148
  v_mfma_f32_16x16x32_bf16 v[20:23], v[92:95], v[52:55], v[20:23]
  v_fma_f32 v13, v13, v219, v149
  v_fma_f32 v14, v14, v219, v150
  v_fma_f32 v15, v15, v219, v151
  v_exp_f32 v144, v8
  v_mfma_f32_16x16x32_bf16 v[16:19], v[92:95], v[48:51], v[16:19]
  v_exp_f32 v145, v9
  v_exp_f32 v146, v10
  v_exp_f32 v147, v11
  v_exp_f32 v148, v12
  v_exp_f32 v149, v13
  v_mfma_f32_16x16x32_bf16 v[20:23], v[104:107], v[36:39], v[20:23]
  v_exp_f32 v150, v14
  v_exp_f32 v151, v15
  v_fma_f32 v144, v144, s72, s72
  v_fma_f32 v145, v145, s72, s72
  v_mfma_f32_16x16x32_bf16 v[16:19], v[104:107], v[32:35], v[16:19]
  v_fma_f32 v146, v146, s72, s72
  v_fma_f32 v147, v147, s72, s72
  v_fma_f32 v148, v148, s72, s72
  v_fma_f32 v149, v149, s72, s72
  v_fma_f32 v150, v150, s72, s72
  v_mfma_f32_16x16x32_bf16 v[28:31], v[84:87], v[44:47], v[28:31]
  v_fma_f32 v151, v151, s72, s72
  v_rcp_f32 v144, v144
  v_rcp_f32 v145, v145
  v_rcp_f32 v146, v146
  v_mfma_f32_16x16x32_bf16 v[24:27], v[84:87], v[40:43], v[24:27]
  v_rcp_f32 v147, v147
  v_rcp_f32 v148, v148
  v_rcp_f32 v149, v149
  v_rcp_f32 v150, v150
  v_rcp_f32 v151, v151
  v_mfma_f32_16x16x32_bf16 v[28:31], v[96:99], v[52:55], v[28:31]
  v_mul_f32 v8, v8, v144
  v_mul_f32 v9, v9, v145
  v_mul_f32 v10, v10, v146
  v_mul_f32 v11, v11, v147
  v_mfma_f32_16x16x32_bf16 v[24:27], v[96:99], v[48:51], v[24:27]
  v_mul_f32 v12, v12, v148
  v_mul_f32 v13, v13, v149
  v_mul_f32 v14, v14, v150
  v_mul_f32 v15, v15, v151
  v_cvt_pk_bf16_f32 v220, v8, v9
  v_mfma_f32_16x16x32_bf16 v[28:31], v[108:111], v[36:39], v[28:31]
  v_cvt_pk_bf16_f32 v221, v10, v11
  v_cvt_pk_bf16_f32 v222, v12, v13
  v_cvt_pk_bf16_f32 v223, v14, v15
  v_min_u32 v220, -2, v220
  v_mfma_f32_16x16x32_bf16 v[24:27], v[108:111], v[32:35], v[24:27]
  v_min_u32 v222, -2, v222
  v_permlane16_swap_b32 v221, v223
  s_nop 0
  v_permlane16_swap_b32 v220, v222
  buffer_store_dwordx4 v[220:223], v6, s[40:43], s24 offen sc1
  v_pk_fma_f32 v[16:17], v[16:17], v[218:219], v[152:153] op_sel:[0,0,0] op_sel_hi:[1,0,1]
  v_pk_fma_f32 v[18:19], v[18:19], v[218:219], v[154:155] op_sel:[0,0,0] op_sel_hi:[1,0,1]
  v_pk_fma_f32 v[20:21], v[20:21], v[218:219], v[156:157] op_sel:[0,1,0] op_sel_hi:[1,1,1]
  v_pk_fma_f32 v[22:23], v[22:23], v[218:219], v[158:159] op_sel:[0,1,0] op_sel_hi:[1,1,1]
  v_exp_f32 v152, v16
  v_exp_f32 v153, v17
  v_exp_f32 v154, v18
  v_exp_f32 v155, v19
  v_exp_f32 v156, v20
  v_exp_f32 v157, v21
  v_exp_f32 v158, v22
  v_exp_f32 v159, v23
  v_pk_fma_f32 v[152:153], v[152:153], s[72:73], s[72:73] op_sel:[0,0,0] op_sel_hi:[1,0,0]
  v_pk_fma_f32 v[154:155], v[154:155], s[72:73], s[72:73] op_sel:[0,0,0] op_sel_hi:[1,0,0]
  v_pk_fma_f32 v[156:157], v[156:157], s[72:73], s[72:73] op_sel:[0,0,0] op_sel_hi:[1,0,0]
  v_pk_fma_f32 v[158:159], v[158:159], s[72:73], s[72:73] op_sel:[0,0,0] op_sel_hi:[1,0,0]
  v_rcp_f32 v152, v152
  v_rcp_f32 v153, v153
  v_rcp_f32 v154, v154
  v_rcp_f32 v155, v155
  v_rcp_f32 v156, v156
  v_rcp_f32 v157, v157
  v_rcp_f32 v158, v158
  v_rcp_f32 v159, v159
  v_pk_mul_f32 v[16:17], v[16:17], v[152:153]
  v_pk_mul_f32 v[18:19], v[18:19], v[154:155]
  v_pk_mul_f32 v[20:21], v[20:21], v[156:157]
  v_pk_mul_f32 v[22:23], v[22:23], v[158:159]
  v_cvt_pk_bf16_f32 v224, v16, v17
  v_cvt_pk_bf16_f32 v225, v18, v19
  v_cvt_pk_bf16_f32 v226, v20, v21
  v_cvt_pk_bf16_f32 v227, v22, v23
  v_min_u32 v224, -2, v224
  v_min_u32 v226, -2, v226
  v_permlane16_swap_b32 v225, v227
  s_nop 0
  v_permlane16_swap_b32 v224, v226
  buffer_store_dwordx4 v[224:227], v6, s[40:43], s25 offen sc1
  v_pk_fma_f32 v[24:25], v[24:25], v[218:219], v[160:161] op_sel:[0,0,0] op_sel_hi:[1,0,1]
  v_pk_fma_f32 v[26:27], v[26:27], v[218:219], v[162:163] op_sel:[0,0,0] op_sel_hi:[1,0,1]
  v_pk_fma_f32 v[28:29], v[28:29], v[218:219], v[164:165] op_sel:[0,1,0] op_sel_hi:[1,1,1]
  v_pk_fma_f32 v[30:31], v[30:31], v[218:219], v[166:167] op_sel:[0,1,0] op_sel_hi:[1,1,1]
  v_exp_f32 v160, v24
  v_exp_f32 v161, v25
  v_exp_f32 v162, v26
  v_exp_f32 v163, v27
  v_exp_f32 v164, v28
  v_exp_f32 v165, v29
  v_exp_f32 v166, v30
  v_exp_f32 v167, v31
  v_pk_fma_f32 v[160:161], v[160:161], s[72:73], s[72:73] op_sel:[0,0,0] op_sel_hi:[1,0,0]
  v_pk_fma_f32 v[162:163], v[162:163], s[72:73], s[72:73] op_sel:[0,0,0] op_sel_hi:[1,0,0]
  v_pk_fma_f32 v[164:165], v[164:165], s[72:73], s[72:73] op_sel:[0,0,0] op_sel_hi:[1,0,0]
  v_pk_fma_f32 v[166:167], v[166:167], s[72:73], s[72:73] op_sel:[0,0,0] op_sel_hi:[1,0,0]
  v_rcp_f32 v160, v160
  v_rcp_f32 v161, v161
  v_rcp_f32 v162, v162
  v_rcp_f32 v163, v163
  v_rcp_f32 v164, v164
  v_rcp_f32 v165, v165
  v_rcp_f32 v166, v166
  v_rcp_f32 v167, v167
  v_pk_mul_f32 v[24:25], v[24:25], v[160:161]
  v_pk_mul_f32 v[26:27], v[26:27], v[162:163]
  v_pk_mul_f32 v[28:29], v[28:29], v[164:165]
  v_pk_mul_f32 v[30:31], v[30:31], v[166:167]
  v_cvt_pk_bf16_f32 v228, v24, v25
  v_cvt_pk_bf16_f32 v229, v26, v27
  v_cvt_pk_bf16_f32 v230, v28, v29
  v_cvt_pk_bf16_f32 v231, v30, v31
  v_min_u32 v228, -2, v228
  v_min_u32 v230, -2, v230
  v_permlane16_swap_b32 v229, v231
  s_nop 0
  v_permlane16_swap_b32 v228, v230
  buffer_store_dwordx4 v[228:231], v6, s[40:43], s26 offen sc1
  s_waitcnt vmcnt(3)
  s_waitcnt lgkmcnt(0)
  s_barrier
  ds_read_b64_tr_b16 v[212:213], v7 offset:33280
  ds_read_b64_tr_b16 v[214:215], v7 offset:35328
  ds_read_b64_tr_b16 v[208:209], v4 offset:33280
  ds_read_b64_tr_b16 v[210:211], v4 offset:35328
  ds_read_b64_tr_b16 v[204:205], v7 offset:32768
  ds_read_b64_tr_b16 v[206:207], v7 offset:34816
  ds_read_b64_tr_b16 v[200:201], v4 offset:32768
  ds_read_b64_tr_b16 v[202:203], v4 offset:34816
  s_add_u32 m0, s96, 0x4000
  s_cmp_ge_u32 s27, 3
  s_cselect_b32 s59, 0x58000, 0
  s_sub_u32 s59, s79, s59
  s_add_u32 s59, s59, 0x40000
  buffer_load_dwordx4 v5, s[20:23], s59 offen lds
  s_add_u32 m0, s96, 0x4400
  s_cmp_ge_u32 s27, 3
  s_cselect_b32 s59, 0x58000, 0
  s_sub_u32 s59, s79, s59
  s_add_u32 s59, s59, 0x44000
  buffer_load_dwordx4 v5, s[20:23], s59 offen lds
  s_add_u32 m0, s96, 0x1e500
  s_cmp_ge_u32 s27, 2
  s_cselect_b32 s59, 0x58000, 0
  s_sub_u32 s59, s79, s59
  s_add_u32 s59, s59, 0x48000
  buffer_load_dwordx4 v5, s[20:23], s59 offen lds
  s_add_u32 m0, s96, 0x1e900
  s_cmp_ge_u32 s27, 2
  s_cselect_b32 s59, 0x58000, 0
  s_sub_u32 s59, s79, s59
  s_add_u32 s59, s59, 0x4c000
  buffer_load_dwordx4 v5, s[20:23], s59 offen lds
  s_add_u32 m0, s96, 0x14500
  s_cmp_ge_u32 s27, 1
  s_cselect_b32 s59, 0x58000, 0
  s_sub_u32 s59, s79, s59
  s_add_u32 s59, s59, 0x50000
  buffer_load_dwordx4 v5, s[20:23], s59 offen lds
  s_add_u32 m0, s96, 0x14900
  s_cmp_ge_u32 s27, 1
  s_cselect_b32 s59, 0x58000, 0
  s_sub_u32 s59, s79, s59
  s_add_u32 s59, s59, 0x54000
  buffer_load_dwordx4 v5, s[20:23], s59 offen lds
  v_add_u32 v244, 0xb000, v1
  s_lshl3_add_u32 s55, s45, s46
  s_mul_i32 s55, s55, 11
  s_add_u32 s55, s55, s27
  s_lshl_b32 s55, s55, 10
  s_cmp_ge_u32 s27, 11
  s_cselect_b32 s56, 0x2c00, 0
  s_sub_u32 s56, s55, s56
  s_add_u32 s65, s56, 0x28000
  s_cmp_ge_u32 s27, 10
  s_cselect_b32 s56, 0x2c00, 0
  s_sub_u32 s56, s55, s56
  s_add_u32 s66, s56, 0x28400
  s_cmp_ge_u32 s27, 9
  s_cselect_b32 s56, 0x2c00, 0
  s_sub_u32 s56, s55, s56
  s_add_u32 s67, s56, 0x28800
  s_cmp_ge_u32 s27, 8
  s_cselect_b32 s56, 0x2c00, 0
  s_sub_u32 s56, s55, s56
  s_add_u32 s68, s56, 0x28c00
  s_cmp_ge_u32 s27, 7
  s_cselect_b32 s56, 0x2c00, 0
  s_sub_u32 s56, s55, s56
  s_add_u32 s69, s56, 0x29000
  s_cmp_ge_u32 s27, 6
  s_cselect_b32 s56, 0x2c00, 0
  s_sub_u32 s56, s55, s56
  s_add_u32 s70, s56, 0x29400
  s_cmp_ge_u32 s27, 5
  s_cselect_b32 s56, 0x2c00, 0
  s_sub_u32 s56, s55, s56
  s_add_u32 s71, s56, 0x29800
  s_cmp_ge_u32 s27, 4
  s_cselect_b32 s56, 0x2c00, 0
  s_sub_u32 s56, s55, s56
  s_add_u32 s72, s56, 0x29c00
  s_cmp_ge_u32 s27, 3
  s_cselect_b32 s56, 0x2c00, 0
  s_sub_u32 s56, s55, s56
  s_add_u32 s73, s56, 0x2a000
  s_cmp_ge_u32 s27, 2
  s_cselect_b32 s56, 0x2c00, 0
  s_sub_u32 s56, s55, s56
  s_add_u32 s74, s56, 0x2a400
  s_cmp_ge_u32 s27, 1
  s_cselect_b32 s56, 0x2c00, 0
  s_sub_u32 s56, s55, s56
  s_add_u32 s75, s56, 0x2a800
  ds_read_b64_tr_b16 v[228:229], v250 offset:57856
  ds_read_b64_tr_b16 v[230:231], v250 offset:59904
  ds_read_b64_tr_b16 v[224:225], v249 offset:57856
  ds_read_b64_tr_b16 v[226:227], v249 offset:59904
  ds_read_b64_tr_b16 v[220:221], v250 offset:57344
  ds_read_b64_tr_b16 v[222:223], v250 offset:59392
  ds_read_b64_tr_b16 v[216:217], v249 offset:57344
  ds_read_b64_tr_b16 v[218:219], v249 offset:59392
  s_xor_b32 s56, s44, 1
  s_mul_i32 s56, s56, 0x1800
  s_add_u32 s56, s56, 0x8000
  v_add_u32 v252, s56, v1
  s_xor_b32 s56, s44, 2
  s_mul_i32 s56, s56, 0x1800
  s_add_u32 s56, s56, 0x8400
  v_add_u32 v253, s56, v1
  s_xor_b32 s56, s44, 3
  s_mul_i32 s56, s56, 0x1800
  s_add_u32 s56, s56, 0x8800
  v_add_u32 v254, s56, v1
  s_mul_i32 s56, s44, 0x1800
  s_add_u32 s56, s56, 0x8000
  v_add_u32 v255, s56, v1
  v_lshrrev_b32 v251, 4, v0
  s_lshl_b32 s55, s46, 5
  s_add_u32 s55, s55, 0x27d80
  v_lshl_add_u32 v251, v251, 3, s55
  ds_read_b64 v[242:243], v251
  buffer_load_dwordx4 v[40:43], v1, s[40:43], s65 offen sc1
  buffer_load_dwordx4 v[44:47], v244, s[40:43], s65 offen sc1
  buffer_load_dwordx4 v[48:51], v1, s[40:43], s66 offen sc1
  buffer_load_dwordx4 v[52:55], v244, s[40:43], s66 offen sc1
  buffer_load_dwordx4 v[56:59], v1, s[40:43], s67 offen sc1
  buffer_load_dwordx4 v[60:63], v244, s[40:43], s67 offen sc1
  buffer_load_dwordx4 v[64:67], v1, s[40:43], s68 offen sc1
  buffer_load_dwordx4 v[68:71], v244, s[40:43], s68 offen sc1
  s_waitcnt lgkmcnt(0)
  v_lshlrev_b32 v240, 16, v242
  v_and_b32 v241, 0xffff0000, v242
  v_lshlrev_b32 v242, 16, v243
  v_and_b32 v243, 0xffff0000, v243
Lck20_%=:
  s_waitcnt vmcnt(6)
  v_max3_u32 v251, v40, v42, v44
  v_max_u32 v251, v251, v46
  v_cmp_lt_u32 vcc, s53, v251
  s_cbranch_vccnz Lsl21_%=
  v_mfma_f32_16x16x32_bf16 v[20:23], v[124:127], v[40:43], 0
  buffer_load_dwordx4 v[72:75], v1, s[40:43], s69 offen sc1
  v_mfma_f32_16x16x32_bf16 v[16:19], v[120:123], v[40:43], 0
  buffer_load_dwordx4 v[76:79], v244, s[40:43], s69 offen sc1
  v_mfma_f32_16x16x32_bf16 v[12:15], v[116:119], v[40:43], 0
  v_mfma_f32_16x16x32_bf16 v[8:11], v[112:115], v[40:43], 0
  v_mfma_f32_16x16x32_bf16 v[36:39], v[124:127], v[44:47], 0
  v_mfma_f32_16x16x32_bf16 v[32:35], v[120:123], v[44:47], 0
Lck22_%=:
  s_waitcnt vmcnt(6)
  v_max3_u32 v251, v48, v50, v52
  v_max_u32 v251, v251, v54
  v_cmp_lt_u32 vcc, s53, v251
  s_cbranch_vccnz Lsl23_%=
  v_mfma_f32_16x16x32_bf16 v[28:31], v[116:119], v[44:47], 0
  v_mfma_f32_16x16x32_bf16 v[24:27], v[112:115], v[44:47], 0
  v_mfma_f32_16x16x32_bf16 v[20:23], v[140:143], v[48:51], v[20:23]
  ds_read_b64_tr_b16 v[124:125], v7 offset:512
  buffer_load_dwordx4 v[80:83], v1, s[40:43], s70 offen sc1
  v_mfma_f32_16x16x32_bf16 v[16:19], v[136:139], v[48:51], v[16:19]
  ds_read_b64_tr_b16 v[126:127], v7 offset:2560
  buffer_load_dwordx4 v[84:87], v244, s[40:43], s70 offen sc1
  v_mfma_f32_16x16x32_bf16 v[12:15], v[132:135], v[48:51], v[12:15]
  ds_read_b64_tr_b16 v[120:121], v4 offset:512
  v_mfma_f32_16x16x32_bf16 v[8:11], v[128:131], v[48:51], v[8:11]
  ds_read_b64_tr_b16 v[122:123], v4 offset:2560
  v_mfma_f32_16x16x32_bf16 v[36:39], v[140:143], v[52:55], v[36:39]
  ds_read_b64_tr_b16 v[116:117], v7
  v_mfma_f32_16x16x32_bf16 v[32:35], v[136:139], v[52:55], v[32:35]
  ds_read_b64_tr_b16 v[118:119], v7 offset:2048
Lck24_%=:
  s_waitcnt vmcnt(6)
  v_max3_u32 v251, v56, v58, v60
  v_max_u32 v251, v251, v62
  v_cmp_lt_u32 vcc, s53, v251
  s_cbranch_vccnz Lsl25_%=
  v_mfma_f32_16x16x32_bf16 v[28:31], v[132:135], v[52:55], v[28:31]
  ds_read_b64_tr_b16 v[112:113], v4
  v_mfma_f32_16x16x32_bf16 v[24:27], v[128:131], v[52:55], v[24:27]
  ds_read_b64_tr_b16 v[114:115], v4 offset:2048
  v_mfma_f32_16x16x32_bf16 v[20:23], v[180:183], v[56:59], v[20:23]
  ds_read_b64_tr_b16 v[140:141], v250 offset:25088
  buffer_load_dwordx4 v[88:91], v1, s[40:43], s71 offen sc1
  v_mfma_f32_16x16x32_bf16 v[16:19], v[176:179], v[56:59], v[16:19]
  ds_read_b64_tr_b16 v[142:143], v250 offset:27136
  buffer_load_dwordx4 v[92:95], v244, s[40:43], s71 offen sc1
  v_mfma_f32_16x16x32_bf16 v[12:15], v[172:175], v[56:59], v[12:15]
  ds_read_b64_tr_b16 v[136:137], v249 offset:25088
  v_mfma_f32_16x16x32_bf16 v[8:11], v[168:171], v[56:59], v[8:11]
  ds_read_b64_tr_b16 v[138:139], v249 offset:27136
  v_mfma_f32_16x16x32_bf16 v[36:39], v[180:183], v[60:63], v[36:39]
  ds_read_b64_tr_b16 v[132:133], v250 offset:24576
  v_mfma_f32_16x16x32_bf16 v[32:35], v[176:179], v[60:63], v[32:35]
  ds_read_b64_tr_b16 v[134:135], v250 offset:26624
Lck26_%=:
  s_waitcnt vmcnt(6)
  v_max3_u32 v251, v64, v66, v68
  v_max_u32 v251, v251, v70
  v_cmp_lt_u32 vcc, s53, v251
  s_cbranch_vccnz Lsl27_%=
  v_mfma_f32_16x16x32_bf16 v[28:31], v[172:175], v[60:63], v[28:31]
  ds_read_b64_tr_b16 v[128:129], v249 offset:24576
  v_mfma_f32_16x16x32_bf16 v[24:27], v[168:171], v[60:63], v[24:27]
  ds_read_b64_tr_b16 v[130:131], v249 offset:26624
  v_mfma_f32_16x16x32_bf16 v[20:23], v[196:199], v[64:67], v[20:23]
  s_barrier
  ds_read_b64_tr_b16 v[180:181], v7 offset:16896
  buffer_load_dwordx4 v[96:99], v1, s[40:43], s72 offen sc1
  v_mfma_f32_16x16x32_bf16 v[16:19], v[192:195], v[64:67], v[16:19]
  ds_read_b64_tr_b16 v[182:183], v7 offset:18944
  buffer_load_dwordx4 v[100:103], v244, s[40:43], s72 offen sc1
  v_mfma_f32_16x16x32_bf16 v[12:15], v[188:191], v[64:67], v[12:15]
  ds_read_b64_tr_b16 v[176:177], v4 offset:16896
  v_mfma_f32_16x16x32_bf16 v[8:11], v[184:187], v[64:67], v[8:11]
  ds_read_b64_tr_b16 v[178:179], v4 offset:18944
  v_mfma_f32_16x16x32_bf16 v[36:39], v[196:199], v[68:71], v[36:39]
  ds_read_b64_tr_b16 v[172:173], v7 offset:16384
  v_mfma_f32_16x16x32_bf16 v[32:35], v[192:195], v[68:71], v[32:35]
  ds_read_b64_tr_b16 v[174:175], v7 offset:18432
Lck28_%=:
  s_waitcnt vmcnt(6)
  v_max3_u32 v251, v72, v74, v76
  v_max_u32 v251, v251, v78
  v_cmp_lt_u32 vcc, s53, v251
  s_cbranch_vccnz Lsl29_%=
  v_mfma_f32_16x16x32_bf16 v[28:31], v[188:191], v[68:71], v[28:31]
  ds_read_b64_tr_b16 v[168:169], v4 offset:16384
  v_mfma_f32_16x16x32_bf16 v[24:27], v[184:187], v[68:71], v[24:27]
  ds_read_b64_tr_b16 v[170:171], v4 offset:18432
  v_mfma_f32_16x16x32_bf16 v[20:23], v[212:215], v[72:75], v[20:23]
  ds_read_b64_tr_b16 v[196:197], v250 offset:41472
  buffer_load_dwordx4 v[104:107], v1, s[40:43], s73 offen sc1
  v_mfma_f32_16x16x32_bf16 v[16:19], v[208:211], v[72:75], v[16:19]
  ds_read_b64_tr_b16 v[198:199], v250 offset:43520
  buffer_load_dwordx4 v[108:111], v244, s[40:43], s73 offen sc1
  v_mfma_f32_16x16x32_bf16 v[12:15], v[204:207], v[72:75], v[12:15]
  ds_read_b64_tr_b16 v[192:193], v249 offset:41472
  v_mfma_f32_16x16x32_bf16 v[8:11], v[200:203], v[72:75], v[8:11]
  ds_read_b64_tr_b16 v[194:195], v249 offset:43520
  v_mfma_f32_16x16x32_bf16 v[36:39], v[212:215], v[76:79], v[36:39]
  ds_read_b64_tr_b16 v[188:189], v250 offset:40960
  v_mfma_f32_16x16x32_bf16 v[32:35], v[208:211], v[76:79], v[32:35]
  ds_read_b64_tr_b16 v[190:191], v250 offset:43008
Lck30_%=:
  s_waitcnt vmcnt(6)
  v_max3_u32 v251, v80, v82, v84
  v_max_u32 v251, v251, v86
  v_cmp_lt_u32 vcc, s53, v251
  s_cbranch_vccnz Lsl31_%=
  v_mfma_f32_16x16x32_bf16 v[28:31], v[204:207], v[76:79], v[28:31]
  ds_read_b64_tr_b16 v[184:185], v249 offset:40960
  v_mfma_f32_16x16x32_bf16 v[24:27], v[200:203], v[76:79], v[24:27]
  ds_read_b64_tr_b16 v[186:187], v249 offset:43008
  v_mfma_f32_16x16x32_bf16 v[20:23], v[228:231], v[80:83], v[20:23]
  ds_read_b64_tr_b16 v[212:213], v250 offset:512
  buffer_load_dwordx4 v[144:147], v1, s[40:43], s74 offen sc1
  v_mfma_f32_16x16x32_bf16 v[16:19], v[224:227], v[80:83], v[16:19]
  ds_read_b64_tr_b16 v[214:215], v250 offset:2560
  buffer_load_dwordx4 v[148:151], v244, s[40:43], s74 offen sc1
  v_mfma_f32_16x16x32_bf16 v[12:15], v[220:223], v[80:83], v[12:15]
  ds_read_b64_tr_b16 v[208:209], v249 offset:512
  v_mfma_f32_16x16x32_bf16 v[8:11], v[216:219], v[80:83], v[8:11]
  ds_read_b64_tr_b16 v[210:211], v249 offset:2560
  v_mfma_f32_16x16x32_bf16 v[36:39], v[228:231], v[84:87], v[36:39]
  ds_read_b64_tr_b16 v[204:205], v250
  v_mfma_f32_16x16x32_bf16 v[32:35], v[224:227], v[84:87], v[32:35]
  ds_read_b64_tr_b16 v[206:207], v250 offset:2048
Lck32_%=:
  s_waitcnt vmcnt(6)
  v_max3_u32 v251, v88, v90, v92
  v_max_u32 v251, v251, v94
  v_cmp_lt_u32 vcc, s53, v251
  s_cbranch_vccnz Lsl33_%=
  v_mfma_f32_16x16x32_bf16 v[28:31], v[220:223], v[84:87], v[28:31]
  ds_read_b64_tr_b16 v[200:201], v249
  v_mfma_f32_16x16x32_bf16 v[24:27], v[216:219], v[84:87], v[24:27]
  ds_read_b64_tr_b16 v[202:203], v249 offset:2048
  s_waitcnt lgkmcnt(15)
  v_mfma_f32_16x16x32_bf16 v[20:23], v[124:127], v[88:91], v[20:23]
  buffer_load_dwordx4 v[152:155], v1, s[40:43], s75 offen sc1
  s_waitcnt lgkmcnt(15)
  v_mfma_f32_16x16x32_bf16 v[16:19], v[120:123], v[88:91], v[16:19]
  buffer_load_dwordx4 v[156:159], v244, s[40:43], s75 offen sc1
  s_waitcnt lgkmcnt(15)
  v_mfma_f32_16x16x32_bf16 v[12:15], v[116:119], v[88:91], v[12:15]
  s_waitcnt lgkmcnt(15)
  v_mfma_f32_16x16x32_bf16 v[8:11], v[112:115], v[88:91], v[8:11]
  v_mfma_f32_16x16x32_bf16 v[36:39], v[124:127], v[92:95], v[36:39]
  v_mfma_f32_16x16x32_bf16 v[32:35], v[120:123], v[92:95], v[32:35]
Lck34_%=:
  s_waitcnt vmcnt(6)
  v_max3_u32 v251, v96, v98, v100
  v_max_u32 v251, v251, v102
  v_cmp_lt_u32 vcc, s53, v251
  s_cbranch_vccnz Lsl35_%=
  v_mfma_f32_16x16x32_bf16 v[28:31], v[116:119], v[92:95], v[28:31]
  v_mfma_f32_16x16x32_bf16 v[24:27], v[112:115], v[92:95], v[24:27]
  s_waitcnt lgkmcnt(15)
  v_mfma_f32_16x16x32_bf16 v[20:23], v[140:143], v[96:99], v[20:23]
  s_waitcnt lgkmcnt(15)
  v_mfma_f32_16x16x32_bf16 v[16:19], v[136:139], v[96:99], v[16:19]
  s_waitcnt lgkmcnt(15)
  v_mfma_f32_16x16x32_bf16 v[12:15], v[132:135], v[96:99], v[12:15]
  s_waitcnt lgkmcnt(15)
  v_mfma_f32_16x16x32_bf16 v[8:11], v[128:131], v[96:99], v[8:11]
  v_mfma_f32_16x16x32_bf16 v[36:39], v[140:143], v[100:103], v[36:39]
  v_mfma_f32_16x16x32_bf16 v[32:35], v[136:139], v[100:103], v[32:35]
Lck36_%=:
  s_waitcnt vmcnt(4)
  v_max3_u32 v251, v104, v106, v108
  v_max_u32 v251, v251, v110
  v_cmp_lt_u32 vcc, s53, v251
  s_cbranch_vccnz Lsl37_%=
  v_mfma_f32_16x16x32_bf16 v[28:31], v[132:135], v[100:103], v[28:31]
  v_mfma_f32_16x16x32_bf16 v[24:27], v[128:131], v[100:103], v[24:27]
  s_waitcnt lgkmcnt(15)
  v_mfma_f32_16x16x32_bf16 v[20:23], v[180:183], v[104:107], v[20:23]
  s_waitcnt lgkmcnt(15)
  v_mfma_f32_16x16x32_bf16 v[16:19], v[176:179], v[104:107], v[16:19]
  s_waitcnt lgkmcnt(15)
  v_mfma_f32_16x16x32_bf16 v[12:15], v[172:175], v[104:107], v[12:15]
  s_waitcnt lgkmcnt(15)
  v_mfma_f32_16x16x32_bf16 v[8:11], v[168:171], v[104:107], v[8:11]
  v_mfma_f32_16x16x32_bf16 v[36:39], v[180:183], v[108:111], v[36:39]
  v_mfma_f32_16x16x32_bf16 v[32:35], v[176:179], v[108:111], v[32:35]
Lck38_%=:
  s_waitcnt vmcnt(2)
  v_max3_u32 v251, v144, v146, v148
  v_max_u32 v251, v251, v150
  v_cmp_lt_u32 vcc, s53, v251
  s_cbranch_vccnz Lsl39_%=
  v_mfma_f32_16x16x32_bf16 v[28:31], v[172:175], v[108:111], v[28:31]
  v_mfma_f32_16x16x32_bf16 v[24:27], v[168:171], v[108:111], v[24:27]
  s_waitcnt lgkmcnt(14)
  v_mfma_f32_16x16x32_bf16 v[20:23], v[196:199], v[144:147], v[20:23]
  s_waitcnt lgkmcnt(12)
  v_mfma_f32_16x16x32_bf16 v[16:19], v[192:195], v[144:147], v[16:19]
  s_waitcnt lgkmcnt(10)
  v_mfma_f32_16x16x32_bf16 v[12:15], v[188:191], v[144:147], v[12:15]
  s_waitcnt lgkmcnt(8)
  v_mfma_f32_16x16x32_bf16 v[8:11], v[184:187], v[144:147], v[8:11]
  v_mfma_f32_16x16x32_bf16 v[36:39], v[196:199], v[148:151], v[36:39]
  v_mfma_f32_16x16x32_bf16 v[32:35], v[192:195], v[148:151], v[32:35]
Lck40_%=:
  s_waitcnt vmcnt(0)
  v_max3_u32 v251, v152, v154, v156
  v_max_u32 v251, v251, v158
  v_cmp_lt_u32 vcc, s53, v251
  s_cbranch_vccnz Lsl41_%=
  v_mfma_f32_16x16x32_bf16 v[28:31], v[188:191], v[148:151], v[28:31]
  v_mfma_f32_16x16x32_bf16 v[24:27], v[184:187], v[148:151], v[24:27]
  s_waitcnt lgkmcnt(6)
  v_mfma_f32_16x16x32_bf16 v[20:23], v[212:215], v[152:155], v[20:23]
  s_waitcnt lgkmcnt(4)
  v_mfma_f32_16x16x32_bf16 v[16:19], v[208:211], v[152:155], v[16:19]
  s_waitcnt lgkmcnt(2)
  v_mfma_f32_16x16x32_bf16 v[12:15], v[204:207], v[152:155], v[12:15]
  s_waitcnt lgkmcnt(0)
  v_mfma_f32_16x16x32_bf16 v[8:11], v[200:203], v[152:155], v[8:11]
  v_mfma_f32_16x16x32_bf16 v[36:39], v[212:215], v[156:159], v[36:39]
  v_mfma_f32_16x16x32_bf16 v[32:35], v[208:211], v[156:159], v[32:35]
  ds_write_b128 v254, v[20:23]
  v_mfma_f32_16x16x32_bf16 v[28:31], v[204:207], v[156:159], v[28:31]
  ds_write_b128 v253, v[16:19]
  v_mfma_f32_16x16x32_bf16 v[24:27], v[200:203], v[156:159], v[24:27]
  ds_write_b128 v252, v[12:15]
  s_nop 1
  ds_write_b128 v254, v[36:39] offset:3072
  ds_write_b128 v253, v[32:35] offset:3072
  s_nop 0
  ds_write_b128 v252, v[28:31] offset:3072
  s_waitcnt lgkmcnt(0)
  s_barrier
  ds_read_b128 v[12:15], v255
  ds_read_b128 v[16:19], v255 offset:1024
  ds_read_b128 v[20:23], v255 offset:2048
  ds_read_b128 v[28:31], v255 offset:3072
  ds_read_b128 v[32:35], v255 offset:4096
  ds_read_b128 v[36:39], v255 offset:5120
  s_waitcnt lgkmcnt(5)
  v_pk_add_f32 v[8:9], v[8:9], v[12:13]
  v_pk_add_f32 v[10:11], v[10:11], v[14:15]
  s_waitcnt lgkmcnt(3)
  v_pk_add_f32 v[16:17], v[16:17], v[20:21]
  v_pk_add_f32 v[18:19], v[18:19], v[22:23]
  v_pk_add_f32 v[8:9], v[8:9], v[16:17]
  v_pk_add_f32 v[10:11], v[10:11], v[18:19]
  v_pk_fma_f32 v[232:233], v[240:241], v[8:9], v[232:233]
  v_pk_fma_f32 v[234:235], v[242:243], v[10:11], v[234:235]
  v_cvt_pk_bf16_f32 v232, v232, v233
  v_cvt_pk_bf16_f32 v233, v234, v235
  s_add_u32 s55, s98, 0
  buffer_store_dwordx2 v[232:233], v248, s[32:35], s55 offen
  s_waitcnt lgkmcnt(2)
  v_pk_add_f32 v[24:25], v[24:25], v[28:29]
  v_pk_add_f32 v[26:27], v[26:27], v[30:31]
  s_waitcnt lgkmcnt(0)
  v_pk_add_f32 v[32:33], v[32:33], v[36:37]
  v_pk_add_f32 v[34:35], v[34:35], v[38:39]
  v_pk_add_f32 v[24:25], v[24:25], v[32:33]
  v_pk_add_f32 v[26:27], v[26:27], v[34:35]
  v_pk_fma_f32 v[236:237], v[240:241], v[24:25], v[236:237]
  v_pk_fma_f32 v[238:239], v[242:243], v[26:27], v[238:239]
  v_cvt_pk_bf16_f32 v236, v236, v237
  v_cvt_pk_bf16_f32 v237, v238, v239
  s_add_u32 s55, s98, 0x8000
  buffer_store_dwordx2 v[236:237], v248, s[32:35], s55 offen
  s_endpgm
Lsl6_%=:
  buffer_load_dwordx2 v[20:21], v5, s[36:39], s72 offen sc1
  buffer_load_dwordx2 v[24:25], v5, s[36:39], s73 offen sc1
  buffer_load_dwordx2 v[26:27], v240, s[36:39], s74 offen sc1
  s_sub_u32 s50, s50, 1
  s_cselect_b64 s[0:1], 0, s[0:1]
  s_cselect_b32 s53, -1, s53
  s_waitcnt vmcnt(0)
  s_branch Lck5_%=
Lsl8_%=:
  buffer_load_dword v200, v201, s[40:43], s74 offen sc1
  s_sub_u32 s50, s50, 1
  s_cselect_b64 s[0:1], 0, s[0:1]
  s_cselect_b32 s53, -1, s53
  s_waitcnt vmcnt(0)
  s_branch Lck7_%=
Lsl10_%=:
  buffer_load_dwordx4 v[200:203], v1, s[40:43], s28 offen sc1
  buffer_load_dwordx4 v[204:207], v1, s[40:43], s28 offen offset:1024 sc1
  buffer_load_dwordx4 v[208:211], v1, s[40:43], s28 offen offset:2048 sc1
  buffer_load_dwordx4 v[212:215], v1, s[40:43], s28 offen offset:3072 sc1
  s_sub_u32 s50, s50, 1
  s_cselect_b64 s[0:1], 0, s[0:1]
  s_cselect_b32 s53, -1, s53
  s_waitcnt vmcnt(0)
  s_branch Lck9_%=
Lsl12_%=:
  buffer_load_dwordx4 v[216:219], v1, s[40:43], s29 offen sc1
  buffer_load_dwordx4 v[220:223], v1, s[40:43], s29 offen offset:1024 sc1
  buffer_load_dwordx4 v[224:227], v1, s[40:43], s29 offen offset:2048 sc1
  buffer_load_dwordx4 v[228:231], v1, s[40:43], s29 offen offset:3072 sc1
  s_sub_u32 s50, s50, 1
  s_cselect_b64 s[0:1], 0, s[0:1]
  s_cselect_b32 s53, -1, s53
  s_waitcnt vmcnt(0)
  s_branch Lck11_%=
Lsl14_%=:
  buffer_load_dwordx4 v[8:11], v1, s[40:43], s28 offen sc1
  buffer_load_dwordx4 v[12:15], v1, s[40:43], s28 offen offset:1024 sc1
  buffer_load_dwordx4 v[16:19], v1, s[40:43], s28 offen offset:2048 sc1
  buffer_load_dwordx4 v[20:23], v1, s[40:43], s28 offen offset:3072 sc1
  s_sub_u32 s50, s50, 1
  s_cselect_b64 s[0:1], 0, s[0:1]
  s_cselect_b32 s53, -1, s53
  s_waitcnt vmcnt(0)
  s_branch Lck13_%=
Lsl17_%=:
  buffer_load_dword v8, v12, s[36:39], s55 offen sc1
  buffer_load_dword v9, v12, s[36:39], s56 offen sc1
  buffer_load_dword v10, v12, s[36:39], s57 offen sc1
  buffer_load_dword v11, v12, s[36:39], s77 offen sc1
  s_sub_u32 s50, s50, 1
  s_cselect_b64 s[0:1], 0, s[0:1]
  s_cselect_b32 s53, -1, s53
  s_waitcnt vmcnt(0)
  s_branch Lck16_%=
Lsl19_%=:
  buffer_load_dwordx4 v[24:27], v1, s[40:43], s29 offen sc1
  buffer_load_dwordx4 v[28:31], v1, s[40:43], s29 offen offset:1024 sc1
  buffer_load_dwordx4 v[32:35], v1, s[40:43], s29 offen offset:2048 sc1
  buffer_load_dwordx4 v[36:39], v1, s[40:43], s29 offen offset:3072 sc1
  buffer_load_dwordx4 v[40:43], v1, s[40:43], s75 offen sc1
  buffer_load_dwordx4 v[44:47], v1, s[40:43], s76 offen sc1
  s_sub_u32 s50, s50, 1
  s_cselect_b64 s[0:1], 0, s[0:1]
  s_cselect_b32 s53, -1, s53
  s_waitcnt vmcnt(0)
  s_branch Lck18_%=
Lsl21_%=:
  buffer_load_dwordx4 v[40:43], v1, s[40:43], s65 offen sc1
  buffer_load_dwordx4 v[44:47], v244, s[40:43], s65 offen sc1
  buffer_load_dwordx4 v[48:51], v1, s[40:43], s66 offen sc1
  buffer_load_dwordx4 v[52:55], v244, s[40:43], s66 offen sc1
  s_sub_u32 s50, s50, 1
  s_cselect_b64 s[0:1], 0, s[0:1]
  s_cselect_b32 s53, -1, s53
  s_waitcnt vmcnt(0)
  s_branch Lck20_%=
Lsl23_%=:
  buffer_load_dwordx4 v[48:51], v1, s[40:43], s66 offen sc1
  buffer_load_dwordx4 v[52:55], v244, s[40:43], s66 offen sc1
  buffer_load_dwordx4 v[56:59], v1, s[40:43], s67 offen sc1
  buffer_load_dwordx4 v[60:63], v244, s[40:43], s67 offen sc1
  s_sub_u32 s50, s50, 1
  s_cselect_b64 s[0:1], 0, s[0:1]
  s_cselect_b32 s53, -1, s53
  s_waitcnt vmcnt(0)
  s_branch Lck22_%=
Lsl25_%=:
  buffer_load_dwordx4 v[56:59], v1, s[40:43], s67 offen sc1
  buffer_load_dwordx4 v[60:63], v244, s[40:43], s67 offen sc1
  buffer_load_dwordx4 v[64:67], v1, s[40:43], s68 offen sc1
  buffer_load_dwordx4 v[68:71], v244, s[40:43], s68 offen sc1
  s_sub_u32 s50, s50, 1
  s_cselect_b64 s[0:1], 0, s[0:1]
  s_cselect_b32 s53, -1, s53
  s_waitcnt vmcnt(0)
  s_branch Lck24_%=
Lsl27_%=:
  buffer_load_dwordx4 v[64:67], v1, s[40:43], s68 offen sc1
  buffer_load_dwordx4 v[68:71], v244, s[40:43], s68 offen sc1
  buffer_load_dwordx4 v[72:75], v1, s[40:43], s69 offen sc1
  buffer_load_dwordx4 v[76:79], v244, s[40:43], s69 offen sc1
  s_sub_u32 s50, s50, 1
  s_cselect_b64 s[0:1], 0, s[0:1]
  s_cselect_b32 s53, -1, s53
  s_waitcnt vmcnt(0)
  s_branch Lck26_%=
Lsl29_%=:
  buffer_load_dwordx4 v[72:75], v1, s[40:43], s69 offen sc1
  buffer_load_dwordx4 v[76:79], v244, s[40:43], s69 offen sc1
  buffer_load_dwordx4 v[80:83], v1, s[40:43], s70 offen sc1
  buffer_load_dwordx4 v[84:87], v244, s[40:43], s70 offen sc1
  s_sub_u32 s50, s50, 1
  s_cselect_b64 s[0:1], 0, s[0:1]
  s_cselect_b32 s53, -1, s53
  s_waitcnt vmcnt(0)
  s_branch Lck28_%=
Lsl31_%=:
  buffer_load_dwordx4 v[80:83], v1, s[40:43], s70 offen sc1
  buffer_load_dwordx4 v[84:87], v244, s[40:43], s70 offen sc1
  buffer_load_dwordx4 v[88:91], v1, s[40:43], s71 offen sc1
  buffer_load_dwordx4 v[92:95], v244, s[40:43], s71 offen sc1
  s_sub_u32 s50, s50, 1
  s_cselect_b64 s[0:1], 0, s[0:1]
  s_cselect_b32 s53, -1, s53
  s_waitcnt vmcnt(0)
  s_branch Lck30_%=
Lsl33_%=:
  buffer_load_dwordx4 v[88:91], v1, s[40:43], s71 offen sc1
  buffer_load_dwordx4 v[92:95], v244, s[40:43], s71 offen sc1
  buffer_load_dwordx4 v[96:99], v1, s[40:43], s72 offen sc1
  buffer_load_dwordx4 v[100:103], v244, s[40:43], s72 offen sc1
  s_sub_u32 s50, s50, 1
  s_cselect_b64 s[0:1], 0, s[0:1]
  s_cselect_b32 s53, -1, s53
  s_waitcnt vmcnt(0)
  s_branch Lck32_%=
Lsl35_%=:
  buffer_load_dwordx4 v[96:99], v1, s[40:43], s72 offen sc1
  buffer_load_dwordx4 v[100:103], v244, s[40:43], s72 offen sc1
  buffer_load_dwordx4 v[104:107], v1, s[40:43], s73 offen sc1
  buffer_load_dwordx4 v[108:111], v244, s[40:43], s73 offen sc1
  s_sub_u32 s50, s50, 1
  s_cselect_b64 s[0:1], 0, s[0:1]
  s_cselect_b32 s53, -1, s53
  s_waitcnt vmcnt(0)
  s_branch Lck34_%=
Lsl37_%=:
  buffer_load_dwordx4 v[104:107], v1, s[40:43], s73 offen sc1
  buffer_load_dwordx4 v[108:111], v244, s[40:43], s73 offen sc1
  buffer_load_dwordx4 v[144:147], v1, s[40:43], s74 offen sc1
  buffer_load_dwordx4 v[148:151], v244, s[40:43], s74 offen sc1
  s_sub_u32 s50, s50, 1
  s_cselect_b64 s[0:1], 0, s[0:1]
  s_cselect_b32 s53, -1, s53
  s_waitcnt vmcnt(0)
  s_branch Lck36_%=
Lsl39_%=:
  buffer_load_dwordx4 v[144:147], v1, s[40:43], s74 offen sc1
  buffer_load_dwordx4 v[148:151], v244, s[40:43], s74 offen sc1
  buffer_load_dwordx4 v[152:155], v1, s[40:43], s75 offen sc1
  buffer_load_dwordx4 v[156:159], v244, s[40:43], s75 offen sc1
  s_sub_u32 s50, s50, 1
  s_cselect_b64 s[0:1], 0, s[0:1]
  s_cselect_b32 s53, -1, s53
  s_waitcnt vmcnt(0)
  s_branch Lck38_%=
Lsl41_%=:
  buffer_load_dwordx4 v[152:155], v1, s[40:43], s75 offen sc1
  buffer_load_dwordx4 v[156:159], v244, s[40:43], s75 offen sc1
  s_sub_u32 s50, s50, 1
  s_cselect_b64 s[0:1], 0, s[0:1]
  s_cselect_b32 s53, -1, s53
  s_waitcnt vmcnt(0)
  s_branch Lck40_%=
)ASM"
               : "+{v0}"(tid), "+{s2}"(bid), "+{s[0:1]}"(ka)
               : "s"(lds)
               : "memory", "v255", "s99", "vcc", "scc");
}

extern "C" void kernel_launch(void* const* d_in, const int* in_sizes, int n_in, void* d_out, int out_size, void* d_ws,
                              size_t ws_size, hipStream_t stream) {
  Args a;
  const uint32_t F = 0x20000;
  a.s[0] = {d_in[0], 2048u * 512u * 2u, F};
  a.s[1] = {d_in[1], 512u * 1536u * 2u, F};
  a.s[2] = {d_in[2], 512u * 512u * 2u, F};
  a.s[3] = {d_in[4], 512u * 1376u * 2u, F};
  a.s[4] = {d_in[5], 1376u * 512u * 2u, F};
  a.s[5] = {d_in[7], 512u * 3072u * 2u, F};
  a.s[6] = {d_in[6], 512u * 2u, F};
  a.s[7] = {d_out, 2048u * 512u * 2u, F};
  a.s[8] = {d_ws, 17039360u, F};
  hipLaunchKernelGGL(fused_block, dim3(256), dim3(512), 0, stream, a);
}
```
